```python
import torch

def _softmax(x, axis=-1):
    return torch.softmax(x, dim=axis)
def _gelu(x):
    return torch.nn.functional.gelu(x, approximate="tanh")
def _silu(x):
    return torch.nn.functional.silu(x)
def _sigmoid(x):
    return torch.sigmoid(x)
def _layernorm(x, axis=-1, eps=1e-5):
    m = x.mean(dim=axis, keepdim=True)
    v = x.var(dim=axis, keepdim=True, unbiased=False)
    return (x - m) / torch.sqrt(v + eps)
def _scatter_add(tgt, idx, src, axis=0):
    return tgt.clone().index_add_(axis, idx, src)
def _segment_sum(src, seg, n):
    out = torch.zeros((n,) + src.shape[1:], dtype=src.dtype)
    return out.index_add_(0, seg, src)
def _pad(x, pw):
    flat = tuple(v for pair in reversed(pw) for v in pair)
    return torch.nn.functional.pad(x, flat)

kernel_name = 'concat_heads__linear_attn_block__ssm_conv_s4d__prompt'

def setup_inputs():

    _w = (lambda a: (lambda t: t.to(torch.bfloat16) if t.is_floating_point() else t)(torch.as_tensor(a)))
    def _A_setup():
        B, S, D = 1, 2048, 448
        return {
            "x": _w((lambda *s: torch.randn(*s, dtype=torch.bfloat16) * 0.05)(B, S, D)),
            "wqkv": _w((lambda *s: torch.randn(*s, dtype=torch.bfloat16) * 0.8)(D, 3 * D)),
            "wo": _w((lambda *s: torch.randn(*s, dtype=torch.bfloat16) * 0.05)(D, D)),
        }
    _A_in = _A_setup()

    def _B_setup():
        Bsz, L, D, N = 1, 2048, 448, 256
        return {
            "u": _w((lambda *s: torch.randn(*s, dtype=torch.bfloat16) * 0.05)(Bsz, L, D)),
            "log_A_real": _w((lambda *s: torch.randn(*s, dtype=torch.bfloat16) * 3.2)(D, N) * 0.5),
            "A_imag": _w((lambda *s: torch.randn(*s, dtype=torch.bfloat16) * 0.05)(D, N) * 3.14),
            "C_real": _w((lambda *s: torch.randn(*s, dtype=torch.bfloat16) * 0.2)(D, N) / N ** 0.5),
            "C_imag": _w((lambda *s: torch.randn(*s, dtype=torch.bfloat16) * 0.4)(D, N) / N ** 0.5),
            "log_dt": _w((lambda *s: torch.randn(*s, dtype=torch.bfloat16) * 0.05)(D) * 0.5 - 4.0),
            "D_skip": _w((lambda *s: torch.randn(*s, dtype=torch.bfloat16) * 0.05)(D)),
        }
    _B_in = _B_setup()

    return {
        "A_x": _A_in["x"],
        "A_wqkv": _A_in["wqkv"],
        "A_wo": _A_in["wo"],
        "B_u": _B_in["u"],
        "B_log_A_real": _B_in["log_A_real"],
        "B_A_imag": _B_in["A_imag"],
        "B_C_real": _B_in["C_real"],
        "B_C_imag": _B_in["C_imag"],
        "B_log_dt": _B_in["log_dt"],
        "B_D_skip": _B_in["D_skip"],
    }

def reference(A_x, A_wqkv, A_wo, B_u, B_log_A_real, B_A_imag, B_C_real, B_C_imag, B_log_dt, B_D_skip):
    def _A_ref(x, wqkv, wo):
        B, S, D = x.shape
        H = 16; Dh = D // H
        qkv = (lambda x, *d: x.permute(*d))((x @ wqkv).reshape(B, S, 3, H, Dh), 2, 0, 3, 1, 4)
        q, k, v = qkv[0], qkv[1], qkv[2]
        phi = lambda t: torch.where(t > 0, t + 1, torch.exp(t)) + 1e-6
        pq, pk = phi(q), phi(k)
        kv = torch.einsum("bhsd,bhse->bhde", pk, v)
        z = torch.einsum("bhsd->bhd", pk)
        num = torch.einsum("bhqd,bhde->bhqe", pq, kv)
        den = torch.einsum("bhqd,bhd->bhq", pq, z)[..., None] + 1e-6
        o = num / den
        return (lambda x, *d: x.permute(*d))(o, 0, 2, 1, 3).reshape(B, S, D) @ wo

    def _B_ref(u, log_A_real, A_imag, C_real, C_imag, log_dt, D_skip):
        L = u.shape[1]
        dt = torch.exp(log_dt)
        A = -torch.exp(log_A_real) + 1j * A_imag
        C = (C_real + 1j * C_imag) * dt[:, None]
        pos = (lambda x, dt: x.to(dt))(torch.arange(L), log_dt.dtype)
        P = torch.exp((A * dt[:, None])[:, :, None] * pos[None, None, :])
        K = 2.0 * torch.einsum("dn,dnl->dl", C, P).real
        L2 = 2 * L
        u_t = (lambda x, *d: x.permute(*d))(u, 0, 2, 1)
        Kf = (lambda x, *a, **k: torch.fft.rfft(x.float(), *a, **k))(K, L2)
        Uf = (lambda x, *a, **k: torch.fft.rfft(x.float(), *a, **k))(u_t, L2)
        y = (lambda x, *a, **k: torch.fft.irfft(x, *a, **k).to(torch.bfloat16))(Uf * Kf[None, :, :], L2)[:, :, :L]
        y = (lambda x, *d: x.permute(*d))(y, 0, 2, 1)
        return y + u * D_skip[None, None, :]

    a = _A_ref(A_x, A_wqkv, A_wo)
    b = _B_ref(B_u, B_log_A_real, B_A_imag, B_C_real, B_C_imag, B_log_dt, B_D_skip)
    return torch.concatenate([a, b], axis=-1)
```

```cpp
#include <hip/hip_runtime.h>
#include <hip/hip_bf16.h>
#include <stdint.h>

typedef unsigned short u16;
__device__ __forceinline__ float bf2f(u16 v) { return __uint_as_float(((unsigned)v) << 16); }
__device__ __forceinline__ u16 f2bf(float f) {
    unsigned u = __float_as_uint(f);
    return (u16)((u + 0x7FFF + ((u >> 16) & 1)) >> 16);
}
__device__ __forceinline__ float rbf(float f) { return bf2f(f2bf(f)); }

#define S_ 2048
#define D_ 448
#define H_ 16
#define DH 28
#define N_ 256

__global__ void k_qkv(const u16* x, const u16* w, u16* qkv) {
    int idx = blockIdx.x * 256 + threadIdx.x;
    if (idx >= S_ * 3 * D_) return;
    int r = idx / (3 * D_), c = idx % (3 * D_);
    float acc = 0;
    for (int k = 0; k < D_; k++) acc += bf2f(x[r * D_ + k]) * bf2f(w[k * 3 * D_ + c]);
    float v = rbf(acc);
    if (c < 2 * D_) {
        float t = v > 0 ? rbf(v + 1.0f) : rbf(expf(v));
        v = rbf(t + 1e-6f);
    }
    qkv[idx] = f2bf(v);
}
__global__ void k_kv(const u16* qkv, float* kv) {
    int idx = blockIdx.x * 256 + threadIdx.x;
    if (idx >= H_ * DH * 29) return;
    int h = idx / (DH * 29), d = (idx / 29) % DH, e = idx % 29;
    float acc = 0;
    for (int s = 0; s < S_; s++) {
        float pk = bf2f(qkv[s * 3 * D_ + D_ + h * DH + d]);
        float v = e < DH ? bf2f(qkv[s * 3 * D_ + 2 * D_ + h * DH + e]) : 1.0f;
        acc += pk * v;
    }
    kv[idx] = rbf(acc);
}
__global__ void k_o(const u16* qkv, const float* kv, u16* o) {
    int idx = blockIdx.x * 256 + threadIdx.x;
    if (idx >= S_ * D_) return;
    int s = idx / D_, c = idx % D_, h = c / DH, e = c % DH;
    float num = 0, den = 0;
    for (int d = 0; d < DH; d++) {
        float pq = bf2f(qkv[s * 3 * D_ + h * DH + d]);
        num += pq * kv[(h * DH + d) * 29 + e];
        den += pq * kv[(h * DH + d) * 29 + 28];
    }
    num = rbf(num);
    den = rbf(rbf(den) + 1e-6f);
    o[idx] = f2bf(num / den);
}
__global__ void k_out(const u16* o, const u16* wo, u16* out) {
    int idx = blockIdx.x * 256 + threadIdx.x;
    if (idx >= S_ * D_) return;
    int r = idx / D_, c = idx % D_;
    float acc = 0;
    for (int k = 0; k < D_; k++) acc += bf2f(o[r * D_ + k]) * bf2f(wo[k * D_ + c]);
    out[r * 2 * D_ + c] = f2bf(acc);
}
__global__ void k_kgen(const u16* lar, const u16* aim, const u16* cr, const u16* ci, const u16* ldt, float* K) {
    int idx = blockIdx.x * 256 + threadIdx.x;
    if (idx >= D_ * S_) return;
    int d = idx / S_, l = idx % S_;
    float dt = rbf(expf(bf2f(ldt[d])));
    float pos = rbf((float)l);
    float acc = 0;
    for (int n = 0; n < N_; n++) {
        float ar = -rbf(expf(bf2f(lar[d * N_ + n]))) * dt;
        float ai = bf2f(aim[d * N_ + n]) * dt;
        float c_r = bf2f(cr[d * N_ + n]) * dt, c_i = bf2f(ci[d * N_ + n]) * dt;
        float e = expf(ar * pos);
        float sn, cs;
        sincosf(ai * pos, &sn, &cs);
        acc += e * (c_r * cs - c_i * sn);
    }
    K[idx] = 2.0f * acc;
}
__global__ void k_conv(const u16* u, const float* K, const u16* dskip, u16* out) {
    int idx = blockIdx.x * 256 + threadIdx.x;
    if (idx >= D_ * S_) return;
    int l = idx / D_, d = idx % D_;
    float acc = 0;
    for (int j = 0; j <= l; j++) acc += K[d * S_ + j] * bf2f(u[(l - j) * D_ + d]);
    float y = rbf(acc);
    float sk = rbf(bf2f(u[l * D_ + d]) * bf2f(dskip[d]));
    out[l * 2 * D_ + D_ + d] = f2bf(y + sk);
}

extern "C" void kernel_launch(void* const* d_in, const int* in_sizes, int n_in,
                              void* d_out, int out_size, void* d_ws, size_t ws_size,
                              hipStream_t stream) {
    const u16* x = (const u16*)d_in[0];
    const u16* wqkv = (const u16*)d_in[1];
    const u16* wo = (const u16*)d_in[2];
    const u16* u = (const u16*)d_in[3];
    const u16* lar = (const u16*)d_in[4];
    const u16* aim = (const u16*)d_in[5];
    const u16* cr = (const u16*)d_in[6];
    const u16* ci = (const u16*)d_in[7];
    const u16* ldt = (const u16*)d_in[8];
    const u16* dsk = (const u16*)d_in[9];
    u16* out = (u16*)d_out;
    char* ws = (char*)d_ws;
    u16* qkv = (u16*)ws;
    float* kv = (float*)(ws + (8 << 20));
    u16* o = (u16*)(ws + (9 << 20));
    float* K = (float*)(ws + (12 << 20));
    k_qkv<<<(S_ * 3 * D_ + 255) / 256, 256, 0, stream>>>(x, wqkv, qkv);
    k_kv<<<(H_ * DH * 29 + 255) / 256, 256, 0, stream>>>(qkv, kv);
    k_o<<<(S_ * D_ + 255) / 256, 256, 0, stream>>>(qkv, kv, o);
    k_out<<<(S_ * D_ + 255) / 256, 256, 0, stream>>>(o, wo, out);
    k_kgen<<<(S_ * D_ + 255) / 256, 256, 0, stream>>>(lar, aim, cr, ci, ldt, K);
    k_conv<<<(S_ * D_ + 255) / 256, 256, 0, stream>>>(u, K, dsk, out);
}
```

```cpp
#include <hip/hip_runtime.h>
#include <stdint.h>
#pragma clang diagnostic ignored "-Wint-to-pointer-cast"

typedef unsigned v4u __attribute__((ext_vector_type(4)));
typedef unsigned v2u __attribute__((ext_vector_type(2)));
typedef float v16f __attribute__((ext_vector_type(16)));
typedef float v4f __attribute__((ext_vector_type(4)));
typedef float v2f __attribute__((ext_vector_type(2)));
typedef __bf16 v8b __attribute__((ext_vector_type(8)));
typedef __bf16 v2b __attribute__((ext_vector_type(2)));
typedef short v4s __attribute__((ext_vector_type(4)));
#define LAS __attribute__((address_space(3)))
#define DI __device__ __forceinline__
typedef __amdgpu_buffer_rsrc_t rsrc_t;

constexpr unsigned OFF_SF = 4096;
constexpr unsigned OFF_PF = 16384;
constexpr unsigned OFF_UF = 32768;
constexpr unsigned OFF_QF = 65536;
constexpr unsigned OFF_PART = 1u << 20;
constexpr unsigned OFF_KV = 2u << 20;
constexpr unsigned OFF_YT = (2u << 20) + 262144;
constexpr unsigned OFF_WP = 4u << 20;
constexpr unsigned OFF_PQ = 5u << 20;
constexpr unsigned OFF_UT = 7u << 20;
constexpr unsigned WS_USED = 9u << 20;
constexpr unsigned L_K = 0, KSTR = 4512, L_U = 36096, USTR = 4224, L_T = 44544, L_S = L_T + 2704, L_X = 47616, L_W = L_X + 58368, L_END = L_W + 57344;
constexpr unsigned S_X = L_S, S_PF = L_S + 8, S_G = L_S + 16, S_V = L_S + 32, S_P = L_S + 40, S_WO = L_S + 48, S_PUB = L_S + 52, S_O = L_S + 64, S_C = L_S + 80,
                   S_TAG = L_S + 96, S_Q = L_S + 112, S_RD = L_S + 128;
constexpr unsigned OSTR = 912;
constexpr unsigned SENT = 0xFFFFFFFFu, TAGC = 0x9E3779B1u;
#define D1 10
#define D2 10
#define D3 10
#define OUTAUX 16
constexpr int SPIN_L = 1 << 20, SPIN_G = 1 << 14;
constexpr int LAG = 8;

#define STAMP(i)

DI float bflo(unsigned v) { return __uint_as_float(v << 16); }
DI float bfhi(unsigned v) { return __uint_as_float(v & 0xffff0000u); }
DI unsigned pk2(float a, float b) { v2f f = {a, b}; return __builtin_bit_cast(unsigned, __builtin_convertvector(f, v2b)); }
DI float rbf(float a) { return bflo(pk2(a, a)); }
DI v16f mfma32(v4u a, v4u b, v16f c) { return __builtin_amdgcn_mfma_f32_32x32x16_bf16(__builtin_bit_cast(v8b, a), __builtin_bit_cast(v8b, b), c, 0, 0, 0); }
DI v4f mfma16(v4u a, v4u b, v4f c) { return __builtin_amdgcn_mfma_f32_16x16x32_bf16(__builtin_bit_cast(v8b, a), __builtin_bit_cast(v8b, b), c, 0, 0, 0); }
DI v4u lr16(unsigned a) { return *(const LAS v4u*)a; }
DI v2u lr8(unsigned a) { return *(const LAS v2u*)a; }
DI unsigned lr4(unsigned a) { return *(const LAS unsigned*)a; }
DI void lw16(unsigned a, v4u v) { *(LAS v4u*)a = v; }
DI void lw8(unsigned a, v2u v) { *(LAS v2u*)a = v; }
DI void lw2(unsigned a, unsigned v) { *(LAS unsigned short*)a = (unsigned short)v; }
DI v2u ltr(unsigned a) { return __builtin_bit_cast(v2u, __builtin_amdgcn_ds_read_tr16_b64_v4i16((LAS v4s*)a)); }
DI v4u ltr2(unsigned a, unsigned b) { v2u x = ltr(a), y = ltr(b); return v4u{x.x, x.y, y.x, y.y}; }
DI void glds(const void* g, unsigned voff, unsigned lds) {
    asm volatile("s_mov_b32 m0, %2\n\ts_nop 0\n\tglobal_load_lds_dwordx4 %0, %1" :: "v"(voff), "s"(g), "s"(lds) : "memory");
}
#define VMWAIT(n) asm volatile("s_waitcnt vmcnt(" #n ")" ::: "memory")
#define CBAR() asm volatile("" ::: "memory")
#define SB() __builtin_amdgcn_sched_barrier(0)
#define PIN(x) asm volatile("" : "+v"(x))
#define NOUNR _Pragma("clang loop unroll(disable)")
#define WGBAR() asm volatile("s_waitcnt lgkmcnt(0)\n\ts_barrier" ::: "memory")
DI v4u wld16(rsrc_t r, unsigned v, unsigned s) { return __builtin_amdgcn_raw_buffer_load_b128(r, v, s, 16); }
DI v2u wld8(rsrc_t r, unsigned v, unsigned s) { return __builtin_amdgcn_raw_buffer_load_b64(r, v, s, 16); }
DI unsigned wld4(rsrc_t r, unsigned v, unsigned s) { return __builtin_amdgcn_raw_buffer_load_b32(r, v, s, 16); }
template <int AUX> DI void st16(rsrc_t r, unsigned v, unsigned s, v4u d) { __builtin_amdgcn_raw_buffer_store_b128(d, r, v, s, AUX); asm volatile("s_nop 1" :: "v"(d) : "memory"); }
DI void wst16(rsrc_t r, unsigned v, unsigned s, v4u d) { st16<16>(r, v, s, d); }
DI void wst4(rsrc_t r, unsigned v, unsigned s, unsigned d) { __builtin_amdgcn_raw_buffer_store_b32(d, r, v, s, 16); }
DI void lset(unsigned a, unsigned v) { CBAR(); *(volatile LAS unsigned*)a = v; CBAR(); }
DI void lwait1(unsigned a, unsigned v) {
    for (int i = 0; i < SPIN_L; i++) { unsigned x = *(volatile LAS unsigned*)a; if (__builtin_amdgcn_readfirstlane(x) >= v) break; }
    CBAR();
}
DI void lwait2(unsigned a, unsigned v) {
    for (int i = 0; i < SPIN_L; i++) { v2u x = *(volatile LAS v2u*)a; if (__builtin_amdgcn_readfirstlane(min(x.x, x.y)) >= v) break; }
    CBAR();
}
DI void lwait4(unsigned a, unsigned v) {
    for (int i = 0; i < SPIN_L; i++) { v4u x = *(volatile LAS v4u*)a; if (__builtin_amdgcn_readfirstlane(min(min(x.x, x.y), min(x.z, x.w))) >= v) break; }
    CBAR();
}
DI unsigned get_tag() {
    v2u x;
    for (int i = 0; i < SPIN_L; i++) { x = *(volatile LAS v2u*)S_TAG; if (__builtin_amdgcn_readfirstlane(x.x) != 0) break; }
    CBAR();
    return __builtin_amdgcn_readfirstlane(x.y);
}
DI unsigned tslice() { return ((unsigned)__builtin_amdgcn_s_memrealtime() >> 4) & 15; }
DI void poll_ts(rsrc_t rw, unsigned vo, unsigned stride, unsigned tag) {
    NOUNR for (int it = 0; it < SPIN_G; it++) { CBAR(); unsigned f = wld4(rw, vo, tslice() * stride); if (__all(f == tag)) break; }
    CBAR();
}
DI void wait_sf(rsrc_t rw, v2u f, int l, unsigned tag) {
    NOUNR for (int it = 0; it < SPIN_G; it++) { if (__all(f.x == tag && f.y == tag)) break; CBAR(); f = wld8(rw, OFF_SF + l * 8, 0); }
    CBAR();
}
DI unsigned mx4(v4u a) { return max(max(a.x, a.y), max(a.z, a.w)); }
DI v4u nosent(v4u a) { return v4u{min(a.x, SENT - 1), min(a.y, SENT - 1), min(a.z, SENT - 1), min(a.w, SENT - 1)}; }
constexpr float TINIT = 1.000001f, LOG2E = 1.44269504088896f;
DI float phiT(float T) { return fmaxf(T, __builtin_amdgcn_fmed3f(__builtin_amdgcn_exp2f(__builtin_fmaf(T, LOG2E, -TINIT * LOG2E)), 0.f, 1.f) + 1e-6f); }
DI v4u unit_sw(unsigned a0, unsigned a1, unsigned b0, unsigned b1) {
    auto r0 = __builtin_amdgcn_permlane32_swap(a0, b0, false, false);
    auto r1 = __builtin_amdgcn_permlane32_swap(a1, b1, false, false);
    return v4u{r0[0], r1[0], r0[1], r1[1]};
}
DI v2f cmul(v2f a, v2f b) { v2f t = v2f{a.x, a.x} * b; v2f bs = {-b.y, b.x}; return __builtin_elementwise_fma(v2f{a.y, a.y}, bs, t); }
DI v2f zpow(float a2, float th, float p) {
    float m = __builtin_amdgcn_exp2f(a2 * p), r = __builtin_amdgcn_fractf(th * p);
    return v2f{m * __builtin_amdgcn_cosf(r), m * __builtin_amdgcn_sinf(r)};
}
DI v16f splat16(float v) { return v16f{v, v, v, v, v, v, v, v, v, v, v, v, v, v, v, v}; }
template <int R0> DI void wrows4(unsigned base, const unsigned* a, const unsigned* b) {
    asm volatile("s_mov_b32 m0, %8\n\ts_nop 0\n\t"
                 "ds_write_addtid_b32 %0 offset:%9\n\tds_write_addtid_b32 %4 offset:%9+256\n\t"
                 "ds_write_addtid_b32 %1 offset:%9+528\n\tds_write_addtid_b32 %5 offset:%9+784\n\t"
                 "ds_write_addtid_b32 %2 offset:%9+1056\n\tds_write_addtid_b32 %6 offset:%9+1312\n\t"
                 "ds_write_addtid_b32 %3 offset:%9+1584\n\tds_write_addtid_b32 %7 offset:%9+1840"
                 :: "v"(a[0]), "v"(a[1]), "v"(a[2]), "v"(a[3]), "v"(b[0]), "v"(b[1]), "v"(b[2]), "v"(b[3]), "s"(base), "i"(R0 * 528) : "memory");
}

__global__ void __launch_bounds__(512, 2)
fused_kernel(const char* __restrict__ x, const char* __restrict__ wqkv, const char* __restrict__ wo, const char* __restrict__ u,
             const char* __restrict__ lar, const char* __restrict__ aim, const char* __restrict__ cre, const char* __restrict__ cim,
             const char* __restrict__ ldt, const char* __restrict__ dsk, char* __restrict__ out, char* ws) {
    __shared__ __attribute__((aligned(16))) char smem[L_END];
    STAMP(0);
    asm volatile("" :: "s"(x), "s"(wqkv), "s"(wo), "s"(u), "s"(lar), "s"(aim), "s"(cre), "s"(cim), "s"(ldt), "s"(dsk), "s"(out), "s"(ws));
    const int b = blockIdx.x, tid = threadIdx.x, l = tid & 63;
    const int w = __builtin_amdgcn_readfirstlane(tid >> 6);
    const int xcd = b & 7, slot = b >> 3;
    const int hp = 4 * (xcd & 1) + 3 - (slot >> 3), c = 8 * (xcd >> 1) + (slot & 7);
    const int lid = hp * 32 + c;
    const bool mainb = hp < 7;
    const int r32 = l & 31, half = l >> 5;
    const rsrc_t rw = __builtin_amdgcn_make_buffer_rsrc(ws, 0, WS_USED, 0x00020000);
    const rsrc_t ro = __builtin_amdgcn_make_buffer_rsrc(out, 0, 2048 * 1792, 0x00020000);
    if ((unsigned)(uintptr_t)smem != 0) return;
    const v4u S4 = {SENT, SENT, SENT, SENT};

    if (w < 4) {
        const int hl = w & 1, isV = w < 2 ? 1 : 0, h = 2 * hp + hl;
        unsigned tag;
        if (w < 2) {
            const unsigned vo = (l >> 3) * 896 + (((l & 7) ^ (4 * w + (l >> 4))) << 4);
            const unsigned vo2 = (l >> 3) * 2688 + (((l & 7) ^ (((l >> 4) & 1) << 2)) << 4);
            const char* gx = x + (64 * c + 8 * w) * 896;
            const char* gw = wqkv + min(hp, 6) * 128 + w * 8 * 2688;
#pragma unroll
            for (int kc = 0; kc < 7; kc++) {
#pragma unroll
                for (int rg = 0; rg < 4; rg++) glds(gx + rg * 16 * 896 + kc * 128, vo, L_X + kc * 8192 + (2 * rg + w) * 1024);
                if (mainb) {
#pragma unroll
                    for (int i = 4 * kc; i < 4 * kc + 4; i++) glds(gw + i * 16 * 2688, vo2, L_W + (2 * i + w) * 1024);
                }
                if (kc == 0) WGBAR();
                if (kc == 5) { if (mainb) VMWAIT(16); else VMWAIT(8); lset(S_X + 4 * w, 1); }
            }
            lset(S_RD + 4 * w, 1);
            tag = get_tag();
            STAMP(1);
        } else if (w == 2) {
            const int lq = l & 7, co = l >> 3;
            v4u ut[8];
            const char* gu = u + (64 * c + 8 * lq) * 896 + min(hp, 6) * 128 + co * 16;
#pragma unroll
            for (int i = 0; i < 8; i++) ut[i] = *(const v4u*)(gu + i * 896);
            CBAR();
            const unsigned E = *(const __attribute__((address_space(4))) unsigned*)ws;
            WGBAR();
            tag = E + TAGC;
            *(volatile LAS v2u*)S_TAG = v2u{1u, tag};
            STAMP(1);
            const unsigned uo = mainb ? OFF_UT + (64 * hp + 8 * co) * 4096 + (8 * c + lq) * 16 : 0x80000000u;
#pragma unroll
            for (int e = 0; e < 8; e++) {
                const unsigned sel = (e & 1) ? 0x07060302u : 0x05040100u;
                wst16(rw, uo, e * 4096, v4u{__builtin_amdgcn_perm(ut[6][e >> 1], ut[7][e >> 1], sel), __builtin_amdgcn_perm(ut[4][e >> 1], ut[5][e >> 1], sel),
                                            __builtin_amdgcn_perm(ut[2][e >> 1], ut[3][e >> 1], sel), __builtin_amdgcn_perm(ut[0][e >> 1], ut[1][e >> 1], sel)});
            }
            CBAR();
            STAMP(2);
        } else {
            const int ph = lid / 28, pk = lid - 28 * ph;
            const unsigned dvo = r32 < 28 ? OFF_WP + half * 448 + r32 * 16 : 0x80000000u;
            const unsigned dso = 2 * ph * 25088 + pk * 896;
            if (mainb) {
                const char* gw = wqkv + 16 * pk * 2688 + 896 + ph * 112;
#pragma unroll
                for (int i = 0; i < 4; i++) { const int q = min(64 * i + l, 223), r = q / 14, p = q - 14 * r; glds(gw, r * 2688 + p * 16 + (p >= 7 ? 784 : 0), L_U + i * 1024); }
#pragma unroll
                for (int uu = 0; uu < 4; uu++) wst16(rw, dvo, dso + (uu >> 1) * 16 * 25088 + (uu & 1) * 25088, S4);
            }
            WGBAR();
            tag = get_tag();
            STAMP(1);
            if (mainb) {
                VMWAIT(0);
                if (l < 16) wst4(rw, OFF_PF + l * 1024 + ph * 128 + pk * 4, 0, tag);
                const unsigned ta = L_U + (8 * half + ((l >> 2) & 3)) * 224 + ((l >> 4) & 1) * 32 + (l & 3) * 8;
                v4u f[4];
#pragma unroll
                for (int uu = 0; uu < 4; uu++) f[uu] = ltr2(ta + uu * 56, ta + uu * 56 + 4 * 224);
#pragma unroll
                for (int uu = 0; uu < 4; uu++) wst16(rw, dvo, dso + (uu >> 1) * 16 * 25088 + (uu & 1) * 25088, nosent(f[uu]));
            }
            STAMP(2);
            poll_ts(rw, OFF_PF + hp * 128 + min(r32, 27) * 4, 1024, tag);
            lset(S_PF, 1);
        }
        lwait1(S_PF, 1);
        lwait2(S_RD, 1);
        STAMP(3);
        const int shift = (!isV && hl) ? 4 : 0;
        const unsigned wvo = OFF_WP + (isV * 16 + h) * 25088 + half * 448 + min(max(r32 - shift, 0), 27) * 16;
        v4u wf[28];
#pragma unroll
        for (int ks = 0; ks < 28; ks++) wf[ks] = wld16(rw, wvo, ks * 896);
        v2u sf = wld8(rw, OFF_SF + l * 8, 0);
        CBAR();
        unsigned xa[4];
#pragma unroll
        for (int m = 0; m < 4; m++) xa[m] = L_X + r32 * 128 + (((2 * m + half) ^ ((l >> 1) & 7)) << 4);
        const int tq = (l >> 2) & 3, tp = l & 3, c16 = (l >> 4) & 1;
        const unsigned trc = L_W + tq * 128 + (((4 * hl + 2 * c16 + (tp >> 1)) ^ ((tq >> 1) << 2)) << 4) + 8 * (tp & 1);
        const unsigned trb = trc + half * 1024;
        const int rh = w >> 1;
        lwait2(S_X, 1);
        STAMP(4);
        v16f q = splat16(TINIT);
        {
            unsigned xq[4];
#pragma unroll
            for (int m = 0; m < 4; m++) xq[m] = xa[m] + rh * 4096;
            v4u fa[28], fb[28];
            if (mainb) {
#pragma unroll
                for (int ks = 0; ks < 16; ks++) {
                    fa[ks] = ltr2(trb + ks * 2048, trb + ks * 2048 + 512); fb[ks] = lr16(xq[ks & 3] + (ks >> 2) * 8192);
                    if (ks >= 3) q = mfma32(fa[ks - 3], fb[ks - 3], q);
                    SB();
                }
            }
            VMWAIT(29);
            if (w < 2) lset(S_X + 4 * w, 2);
            else if (w == 2 && l < 16) wst4(rw, OFF_UF + l * 1024 + lid * 4, 0, tag);
            lwait2(S_X, 2);
            if (mainb) {
#pragma unroll
                for (int ks = 16; ks < 31; ks++) {
                    if (ks < 28) { fa[ks] = ltr2(trb + ks * 2048, trb + ks * 2048 + 512); fb[ks] = lr16(xq[ks & 3] + (ks >> 2) * 8192); }
                    q = mfma32(fa[ks - 3], fb[ks - 3], q);
                    SB();
                }
            }
        }
        STAMP(5);
        const unsigned pqo = mainb ? OFF_PQ + l * 16 : 0x80000000u;
        const float ini = isV ? 0.f : TINIT;
        v16f a0, a1;
        v4u p1[28];
        NOUNR for (int it = 0; it < SPIN_G; it++) { CBAR();
            a0 = splat16(ini); a1 = splat16(ini);
            asm volatile("" : "+v"(q));
            unsigned m = 0;
            v4u p0[28];
            unsigned qp[8];
            p0[0] = lr16(xa[0]); p0[1] = lr16(xa[1]);
            SB();
#pragma unroll
            for (int ks = 0; ks < 28; ks++) {
                if (ks + 2 < 28) p0[ks + 2] = lr16(xa[(ks + 2) & 3] + ((ks + 2) >> 2) * 8192);
                if (ks + 2 >= LAG) p1[ks + 2 - LAG] = lr16(xa[(ks + 2 - LAG) & 3] + 4096 + ((ks + 2 - LAG) >> 2) * 8192);
                a0 = mfma32(p0[ks], wf[ks], a0);
                if (ks >= LAG) a1 = mfma32(p1[ks - LAG], wf[ks - LAG], a1);
                m = max(m, mx4(wf[ks]));
                if (ks >= LAG && ks < LAG + 8) qp[ks - LAG] = pk2(phiT(q[2 * (ks - LAG)]), phiT(q[2 * (ks - LAG) + 1]));
                if (ks == LAG + 4 || ks == LAG + 8) {
                    const int s = ks == LAG + 8;
                    wst16(rw, pqo, (2 * c + rh) * 28672 + (8 * hp + 4 * hl + 2 * s) * 512, unit_sw(qp[4 * s], qp[4 * s + 1], qp[4 * s + 2], qp[4 * s + 3]));
                }
                SB();
            }
            if (__all(m != SENT)) break;
#pragma unroll
            for (int ks = 0; ks < 28; ks++) wf[ks] = wld16(rw, wvo, ks * 896);
        }
        lset(S_G + 4 * w, 1);
        STAMP(6);
        if (isV) {
            unsigned vf[16];
#pragma unroll
            for (int j = 0; j < LAG; j++) {
                const int ks = 28 - LAG + j;
                if (ks + 2 < 28) p1[ks + 2] = lr16(xa[(ks + 2) & 3] + 4096 + ((ks + 2) >> 2) * 8192);
                a1 = mfma32(p1[ks], wf[ks], a1);
                vf[j] = r32 == 28 ? 0x3F803F80u : pk2(a0[2 * j], a0[2 * j + 1]);
                PIN(vf[j]);
                SB();
            }
            wait_sf(rw, sf, l, tag);
            lwait4(S_G, 1);
#pragma unroll
            for (int j = 0; j < 8; j++) vf[8 + j] = r32 == 28 ? 0x3F803F80u : pk2(a1[2 * j], a1[2 * j + 1]);
#pragma unroll
            for (int s = 0; s < 4; s++) lw16(L_X + hl * 4096 + s * 1024 + l * 16, v4u{vf[4 * s], vf[4 * s + 1], vf[4 * s + 2], vf[4 * s + 3]});
            lset(S_V + 4 * hl, 1);
            VMWAIT(0);
            lset(S_Q + 4 * w, 1);
            if (w == 1 && mainb) {
                const unsigned vo2 = (l >> 3) * 896 + (((l & 7) ^ (((l >> 4) & 1) << 2)) << 4);
                const char* gw = wo + hp * 128;
#pragma unroll
                for (int i = 0; i < 56; i++) {
                    glds(gw + i * 8 * 896, vo2, L_W + i * 1024);
                    if (i == 15) { lwait4(S_Q, 1); if (l < 16) wst4(rw, OFF_QF + l * 4096 + c * 128 + hp * 4, 0, tag); }
                }
                VMWAIT(0);
                lset(S_WO, 1);
            }
        } else {
            unsigned kf[16];
#pragma unroll
            for (int j = 0; j < LAG; j++) {
                const int ks = 28 - LAG + j;
                if (ks + 2 < 28) p1[ks + 2] = lr16(xa[(ks + 2) & 3] + 4096 + ((ks + 2) >> 2) * 8192);
                a1 = mfma32(p1[ks], wf[ks], a1);
                kf[j] = pk2(phiT(a0[2 * j]), phiT(a0[2 * j + 1]));
                PIN(kf[j]);
                SB();
            }
            VMWAIT(0);
            lset(S_Q + 4 * w, 1);
#pragma unroll
            for (int j = 0; j < 8; j++) { kf[8 + j] = pk2(phiT(a1[2 * j]), phiT(a1[2 * j + 1])); PIN(kf[8 + j]); }
            lwait1(S_V + 4 * hl, 1);
            v4u vv[4];
#pragma unroll
            for (int s = 0; s < 4; s++) vv[s] = lr16(L_X + hl * 4096 + s * 1024 + l * 16);
            v16f d = splat16(0.f);
#pragma unroll
            for (int s = 0; s < 4; s++) d = mfma32(v4u{kf[4 * s], kf[4 * s + 1], kf[4 * s + 2], kf[4 * s + 3]}, vv[s], d);
            const unsigned mo = (hl && !half) ? 0u : ~0u, me = (!hl && half) ? 0u : ~0u;
            const unsigned po = OFF_PART + ((4 * h + half) * 4 + (r32 >> 3)) * 4096 + c * 128 + (l & 7) * 16;
            wst16(rw, po, 0, nosent(unit_sw(pk2(d[0], d[1]) & mo, pk2(d[2], d[3]) & mo, pk2(d[4], d[5]), pk2(d[6], d[7]))));
            wst16(rw, po, 8 * 4096, nosent(unit_sw(pk2(d[8], d[9]), pk2(d[10], d[11]), pk2(d[12], d[13]) & me, pk2(d[14], d[15]) & me)));
            lset(S_P + 4 * hl, 1);
        }
        STAMP(7);
        if (w == 0) {
            lwait2(S_P, 1);
            __builtin_amdgcn_s_sleep(D1);
            v4u a[4] = {S4, S4, S4, S4};
            NOUNR for (int it = 0; it < SPIN_G; it++) { CBAR();
                unsigned m = 0;
#pragma unroll
                for (int j = 0; j < 4; j++) if (mx4(a[j]) == SENT) a[j] = wld16(rw, OFF_PART + l * 16 + j * 1024, lid * 4096);
                lset(S_RD, 2);
#pragma unroll
                for (int j = 0; j < 4; j++) m = max(m, mx4(a[j]));
                if (__all(m != SENT)) break;
            }
            STAMP(8);
            float s[8];
#pragma unroll
            for (int i = 0; i < 4; i++) {
                s[2 * i] = (bflo(a[0][i]) + bflo(a[1][i])) + (bflo(a[2][i]) + bflo(a[3][i]));
                s[2 * i + 1] = (bfhi(a[0][i]) + bfhi(a[1][i])) + (bfhi(a[2][i]) + bfhi(a[3][i]));
            }
            float t[4], v[2];
#pragma unroll
            for (int i = 0; i < 4; i++) { auto r = __builtin_amdgcn_permlane32_swap(__float_as_uint(s[i]), __float_as_uint(s[i + 4]), false, false); t[i] = __uint_as_float(r[0]) + __uint_as_float(r[1]); }
#pragma unroll
            for (int i = 0; i < 2; i++) { auto r = __builtin_amdgcn_permlane16_swap(__float_as_uint(t[i]), __float_as_uint(t[i + 2]), false, false); v[i] = __uint_as_float(r[0]) + __uint_as_float(r[1]); }
#pragma unroll
            for (int i = 0; i < 2; i++) v[i] += __uint_as_float(__builtin_amdgcn_update_dpp(0, __float_as_uint(v[i]), 0x128, 0xf, 0xf, false));
            const unsigned pv = min(pk2(v[0], v[1]), SENT - 1);
            const unsigned ko = OFF_KV + ((l >> 3) & 1) * 32768 + lid * 128 + (l & 7) * 16 + half * 8 + ((l >> 4) & 1) * 4;
#pragma unroll
            for (int i = 0; i < 4; i++) wst4(rw, ko, i * 65536, pv);
            lset(S_PUB, 1);
            STAMP(9);
        }
        if (mainb) {
            poll_ts(rw, OFF_QF + c * 128 + min(l, 6) * 4, 4096, tag);
            if (lid == 0) poll_ts(rw, OFF_UF + w * 256 + l * 4, 1024, tag);
            lwait1(S_RD, 2);
            v4u pq[16];
#pragma unroll
            for (int i = 0; i < 16; i++) {
                const int pp = i >> 3, hh = (i >> 2) & 1, s = (i >> 1) & 1, rt = i & 1;
                pq[i] = wld16(rw, OFF_PQ + l * 16 + (7 * pp + 3 * hh + 2 * s) * 512, (2 * c + rt) * 28672 + 14 * w * 512);
            }
            CBAR();
            STAMP(10);
            lwait1(S_WO, 1);
            v4u of[28];
            {
                const unsigned t4 = trc + half * 512, t24 = trc + half * 24 * 128, t56 = trc + half * 56 * 128;
#pragma unroll
                for (int g = 0; g < 4; g++) {
                    const unsigned o = g * 112 * 128;
#pragma unroll
                    for (int p = 0; p < 2; p++) {
                        const unsigned op = o + p * 56 * 128;
                        of[7 * g + 3 * p + 0] = ltr2(t4 + op, t4 + op + 8 * 128);
                        of[7 * g + 3 * p + 1] = ltr2(t4 + op + 28 * 128, t4 + op + 36 * 128);
                        of[7 * g + 3 * p + 2] = ltr2(t4 + op + 16 * 128, t24 + op + 24 * 128);
                    }
                    of[7 * g + 6] = ltr2(t56 + o + 44 * 128, t56 + o + 52 * 128);
                }
            }
            lwait1(S_PUB, 1);
            __builtin_amdgcn_s_sleep(D2);
            STAMP(11);
            v4u kv[8] = {S4, S4, S4, S4, S4, S4, S4, S4};
            unsigned so = w * 8192;
            NOUNR for (int it = 0; it < SPIN_G; it++) { CBAR();
                unsigned m = 0;
#pragma unroll
                for (int j = 0; j < 8; j++) if (mx4(kv[j]) == SENT) kv[j] = wld16(rw, OFF_KV + l * 16 + j * 1024, so);
#pragma unroll
                for (int j = 0; j < 8; j++) m = max(m, mx4(kv[j]));
                if (__all(m != SENT)) break;
                so = (so + 32768) & 262143;
            }
            STAMP(12);
            {
                v16f n[8];
                unsigned keep[4];
                const unsigned oa = L_X + r32 * OSTR + w * 224 + half * 16;
                const unsigned ya0 = L_X + r32 * OSTR + (half ? 896 : w * 224 + 192), ya1 = L_X + r32 * OSTR + (half ? 896 : w * 224 + 208);
#pragma unroll
                for (int j = 0; j < 9; j++) {
                    if (j < 8) {
                        const int rt = j >> 2, hh = j & 3;
                        n[j] = mfma32(kv[2 * hh], pq[4 * hh + rt], splat16(0.f));
                        n[j] = mfma32(kv[2 * hh + 1], pq[4 * hh + 2 + rt], n[j]);
                    }
                    if (j > 0) {
                        const int rt = (j - 1) >> 2, hh = (j - 1) & 3;
                        const v16f a = n[j - 1];
                        auto r = __builtin_amdgcn_permlane32_swap(__float_as_uint(a[12]), __float_as_uint(a[12]), false, false);
                        const float rc = __builtin_amdgcn_rcpf(__uint_as_float(r[1]) + 1e-6f);
                        unsigned o[8];
#pragma unroll
                        for (int i = 0; i < 8; i++) o[i] = pk2(a[2 * i] * rc, a[2 * i + 1] * rc);
                        const unsigned ob = oa + rt * 32 * OSTR + (hh >> 1) * 96;
                        lw16(ob + (hh & 1) * 32, v4u{o[0], o[1], o[2], o[3]});
                        if (hh & 1) {
                            lw16(ob + 64, v4u{keep[0], keep[1], half ? o[4] : keep[2], half ? o[5] : keep[3]});
                            lw16((hh & 2 ? ya1 : ya0) + rt * 32 * OSTR, v4u{o[4], o[5], o[6], o[7]});
                        } else {
#pragma unroll
                            for (int i = 0; i < 4; i++) keep[i] = o[4 + i];
                        }
                    }
                    SB();
                }
            }
            lset(S_O + 4 * w, 1);
            lwait4(S_O, 1);
            STAMP(13);
            if (lid == 0 && w == 0 && l == 0) wst4(rw, 0, 0, tag - TAGC + 1);
            v16f acc = splat16(0.f);
            {
                const unsigned ob = L_X + (32 * rh + r32) * OSTR + half * 16;
                v4u fo[28];
#pragma unroll
                for (int ks = 0; ks < 32; ks++) {
                    if (ks < 28) fo[ks] = lr16(ob + ks * 32);
                    if (ks >= 4) acc = mfma32(of[ks - 4], fo[ks - 4], acc);
                    SB();
                }
            }
#pragma unroll
            for (int s = 0; s < 2; s++)
                st16<OUTAUX>(ro, (32 * rh + r32) * 1792 + hl * 64 + s * 32 + half * 16, 64 * c * 1792 + hp * 128,
                             unit_sw(pk2(acc[8 * s], acc[8 * s + 1]), pk2(acc[8 * s + 2], acc[8 * s + 3]), pk2(acc[8 * s + 4], acc[8 * s + 5]), pk2(acc[8 * s + 6], acc[8 * s + 7])));
            STAMP(14);
        }
    } else if (!mainb) {
        const int sw = 4 * c + w - 4;
#pragma unroll
        for (int i = 0; i < 24; i++) { wst16(rw, OFF_PART + l * 16 + i * 1024, sw * 24576, S4); if (i == 3) { if (w == 7 && l < 16) lw16(L_T + 2688 + l * 16, v4u{0u, 0u, 0u, 0u}); WGBAR(); } }
        const unsigned tag = get_tag();
        STAMP(1);
        VMWAIT(0);
        if (l == 0) wst4(rw, OFF_SF + sw * 4, 0, tag);
        STAMP(2);
    } else {
        const int w4 = w - 4, chl = w4 & 1, mh = w4 >> 1, ch = 2 * lid + chl;
        const unsigned po = ch * 512 + mh * 256 + l * 4;
        const unsigned p_ar = *(const unsigned*)(lar + po), p_ai = *(const unsigned*)(aim + po), p_cr = *(const unsigned*)(cre + po), p_ci = *(const unsigned*)(cim + po);
        const unsigned p_dt = *(const unsigned short*)(ldt + ch * 2), p_d = *(const unsigned short*)(dsk + ch * 2);
        CBAR();
        if (w == 7 && l < 16) lw16(L_T + 2688 + l * 16, v4u{0u, 0u, 0u, 0u});
        WGBAR();
        unsigned ga[11];
#pragma unroll
        for (int s = 0; s < 11; s++) {
            const int ll = 8 * (64 * w4 + l) - 3 + s;
            const int code = (int)(pk2((float)ll, 0.f) & 0xffffu);
            const int idx = ll < 0 ? 672 : max(code - 0x4280, min(ll, 128));
            ga[s] = L_T + idx * 4;
        }
        const int g = l >> 4, lov = (l & 15) + 16 * mh;
        const unsigned tw0 = L_T + chl * 2 + 4 * (g < 2 ? 4 * g + 8 * lov : (g == 2 ? 256 + 4 * lov : 384 + 2 * lov));
        const unsigned tw0b = tw0 + (g == 3 ? 248 : 0);
        const unsigned tw1 = L_T + chl * 2 + 4 * (g < 2 ? 512 + 128 * g + lov : 673), tw1b = g == 0 ? tw1 : L_T + 4 * 673 - 128;
#pragma unroll
        for (int s = 0; s < 11; s++) asm volatile("" : "+v"(ga[s]));
        const unsigned tag = get_tag();
        STAMP(1);
        {
            const float dt = rbf(__expf(bflo(p_dt)));
            v2f z1[2], z8[2], z256[2], cc[2];
#pragma unroll
            for (int m = 0; m < 2; m++) {
                const float ar = -rbf(__expf(m ? bfhi(p_ar) : bflo(p_ar))) * dt, ai = (m ? bfhi(p_ai) : bflo(p_ai)) * dt;
                const float a2 = ar * LOG2E, th = ai * 0.159154943091895f;
                z8[m] = zpow(a2, th, 8.f);
                z1[m] = zpow(a2, -th, 1.f);
                z256[m] = zpow(a2, -th, 256.f);
                cc[m] = v2f{(m ? bfhi(p_cr) : bflo(p_cr)) * dt * 2.f, (m ? bfhi(p_ci) : bflo(p_ci)) * dt * -2.f};
            }
            STAMP(2);
            const unsigned sb = L_K + w4 * 8448, sr = sb + (l & 15) * 528 + (l >> 4) * 16;
            v4u lo[2][8];
            v2f g0 = {1.f, 0.f}, g1 = {1.f, 0.f};
            unsigned pa[8], pb[8];
#define LOQ(R0, LAST) \
            for (int r = 0; r < 8; r++) { pa[r] = pk2(g0.x, g0.y); pb[r] = pk2(g1.x, g1.y); if (!(LAST && r == 7)) { g0 = cmul(g0, z8[0]); g1 = cmul(g1, z8[1]); } } \
            wrows4<R0>(sb, pa, pb); wrows4<R0 + 4>(sb, pa + 4, pb + 4);
#pragma unroll
            LOQ(0, false)
#pragma unroll
            LOQ(8, false)
#pragma unroll
            for (int ks = 0; ks < 8; ks++) lo[0][ks] = lr16(sr + ks * 64);
            SB();
#pragma unroll
            LOQ(0, false)
#pragma unroll
            LOQ(8, true)
#pragma unroll
            for (int ks = 0; ks < 8; ks++) lo[1][ks] = lr16(sr + ks * 64);
            SB();
            v2f hh[2][21];
            unsigned hq[2][24];
#pragma unroll
            for (int m = 0; m < 2; m++) {
                hh[m][0] = cc[m];
#pragma unroll
                for (int r = 1; r < 8; r++) hh[m][r] = cmul(hh[m][r - 1], z1[m]);
#pragma unroll
                for (int r = 8; r < 12; r++) hh[m][r] = cmul(hh[m][2 * (r - 8)], z256[m]);
                hh[m][12] = cmul(hh[m][8], z256[m]); hh[m][13] = cmul(hh[m][10], z256[m]);
                hh[m][14] = cmul(hh[m][12], z256[m]); hh[m][15] = cmul(hh[m][13], z256[m]);
#pragma unroll
                for (int r = 0; r < 16; r++) hq[m][r] = pk2(hh[m][r].x, hh[m][r].y);
            }
            wrows4<0>(sb, hq[0], hq[1]); wrows4<4>(sb, hq[0] + 4, hq[1] + 4); wrows4<8>(sb, hq[0] + 8, hq[1] + 8); wrows4<12>(sb, hq[0] + 12, hq[1] + 12);
            v4u fa[8];
#pragma unroll
            for (int ks = 0; ks < 8; ks++) fa[ks] = lr16(sr + ks * 64);
            SB();
#pragma unroll
            for (int m = 0; m < 2; m++) {
                hh[m][16] = cmul(hh[m][14], z256[m]);
#pragma unroll
                for (int r = 17; r < 21; r++) hh[m][r] = cmul(hh[m][r - 1], z256[m]);
#pragma unroll
                for (int r = 16; r < 24; r++) hq[m][r] = pk2(hh[m][min(r, 20)].x, hh[m][min(r, 20)].y);
            }
            wrows4<0>(sb, hq[0] + 16, hq[1] + 16); wrows4<4>(sb, hq[0] + 20, hq[1] + 20);
            v4u fb[8];
#pragma unroll
            for (int ks = 0; ks < 8; ks++) fb[ks] = lr16(sr + ks * 64);
            SB();
            v4f d[2][2];
#pragma unroll
            for (int i = 0; i < 4; i++) d[i >> 1][i & 1] = v4f{0, 0, 0, 0};
#pragma unroll
            for (int ks = 0; ks < 8; ks++) { d[0][0] = mfma16(fa[ks], lo[0][ks], d[0][0]); d[0][1] = mfma16(fa[ks], lo[1][ks], d[0][1]); }
#pragma unroll
            for (int ks = 0; ks < 8; ks++) { d[1][0] = mfma16(fb[ks], lo[0][ks], d[1][0]); d[1][1] = mfma16(fb[ks], lo[1][ks], d[1][1]); }
            lw16(sb + l * 16, __builtin_bit_cast(v4u, mh ? d[0][0] : d[0][1]));
            lw16(sb + 1024 + l * 16, __builtin_bit_cast(v4u, mh ? d[1][0] : d[1][1]));
            lset(S_C + 4 * w4, 1);
            STAMP(3);
            lwait1(S_C + 4 * (w4 ^ 2), 1);
            const unsigned pbase = L_K + (w4 ^ 2) * 8448 + l * 16;
            v4f e0 = __builtin_bit_cast(v4f, lr16(pbase)) + (mh ? d[0][1] : d[0][0]);
            v4f e1 = __builtin_bit_cast(v4f, lr16(pbase + 1024)) + (mh ? d[1][1] : d[1][0]);
            if (mh == 0 && l == 0) e0[0] += bflo(p_d);
            const unsigned k0 = pk2(e0[0], e0[1]), k1 = pk2(e0[2], e0[3]), k2 = pk2(e1[0], e1[1]), k3 = pk2(e1[2], e1[3]);
            lw2(tw0, k0); lw2(tw0 + 4, k0 >> 16); lw2(tw0b + 8, k1); lw2(tw0b + 12, k1 >> 16);
            lw2(tw1, k2); lw2(tw1b + 128, k2 >> 16); lw2(g == 0 ? tw1 + 256 : tw1b + 128, k3); lw2(g == 0 ? tw1 + 384 : tw1b + 128, k3 >> 16);
        }
        lset(S_C + 4 * w4, 2);
        poll_ts(rw, OFF_UF + (lid >> 5) * 128 + r32 * 4, 1024, tag);
        v4u ug[2];
#pragma unroll
        for (int i = 0; i < 2; i++) ug[i] = wld16(rw, OFF_UT + l * 16 + i * 1024, ch * 4096 + mh * 2048);
        v2u sf = wld8(rw, OFF_SF + l * 8, 0);
        STAMP(4);
        lwait4(S_C, 2);
        {
            unsigned t[11];
#pragma unroll
            for (int s = 0; s < 11; s++) t[s] = lr4(ga[s]);
            const unsigned ka = L_K + 384 + (64 * w4 + l) * 16;
#pragma unroll
            for (int cc = 0; cc < 2; cc++) {
                unsigned P[10];
#pragma unroll
                for (int s = 0; s < 10; s++) P[s] = __builtin_amdgcn_perm(t[s + 1], t[s], cc ? 0x07060302u : 0x05040100u);
#pragma unroll
                for (int r = 0; r < 4; r++) lw16(ka + (cc * 4 + r) * KSTR, v4u{P[r], P[r + 2], P[r + 4], P[r + 6]});
            }
            if (l < 48) lw16(L_K + (2 * w4 + l / 24) * KSTR + (l % 24) * 16, v4u{0u, 0u, 0u, 0u});
        }
        STAMP(5);
        lwait1(S_PF, 1);
#pragma unroll
        for (int i = 0; i < 2; i++) lw16(L_U + chl * USTR + 4080 - (128 * mh + 64 * i + l) * 16, ug[i]);
        if (mh == 0 && l < 8) lw16(L_U + chl * USTR + 4096 + l * 16, v4u{0u, 0u, 0u, 0u});
        lset(S_C + 4 * w4, 3);
        lwait4(S_C, 3);
        STAMP(6);
        v4f y[8];
#pragma unroll
        for (int t = 0; t < 8; t++) y[t] = v4f{0, 0, 0, 0};
        {
            const unsigned ab = L_K + (chl * 4 + (l & 3)) * KSTR + 128 * ((l >> 2) & 3) + 16 * (l >> 4) + 256 * mh;
            const unsigned bb = L_U + chl * USTR + 2 * (2076 - 224) - 3584 - 8 * (l & 15) + 16 * (l >> 4) + 256 * mh;
            unsigned bb8 = bb + 8;
            asm volatile("" : "+v"(bb8));
            v4u A[5][8];
            v2u Bp[5][8], Bq[5][8];
#pragma unroll
            for (int rho = -1; rho < 4; rho++) {
                int n = 0;
#pragma unroll
                for (int s = 0; s < 8; s++)
#pragma unroll
                    for (int a = 0; a <= s; a++) {
                        if (rho < 3 && n < 24) {
                            const int i = n / 3, k = n % 3, o = 64 * (rho + 1);
                            if (k == 0) A[rho + 2][i] = lr16(ab + 512 * i + o);
                            else if (k == 1) Bp[rho + 2][i] = lr8(bb + 512 * (7 - i) + o);
                            else Bq[rho + 2][i] = lr8(bb8 + 512 * (7 - i) + o);
                        }
                        n++;
                        if (rho >= 0) {
                            const int bq = s - a;
                            y[s] = mfma16(A[rho + 1][a], v4u{Bp[rho + 1][bq].x, Bp[rho + 1][bq].y, Bq[rho + 1][bq].x, Bq[rho + 1][bq].y}, y[s]);
                        }
                        SB();
                    }
            }
        }
        lset(S_C + 4 * w4, 4);
        STAMP(7);
        wait_sf(rw, sf, l, tag);
        lwait4(S_C, 4);
#pragma unroll
        for (int i = 0; i < 4; i++) lw16(L_K + w4 * 4096 + i * 1024 + l * 16, __builtin_bit_cast(v4u, mh ? y[i] : y[4 + i]));
        lset(S_C + 4 * w4, 5);
        lwait1(S_C + 4 * (w4 ^ 2), 5);
        {
            unsigned o[4][2];
#pragma unroll
            for (int i = 0; i < 4; i++) {
                v4f v = __builtin_bit_cast(v4f, lr16(L_K + (w4 ^ 2) * 4096 + i * 1024 + l * 16)) + (mh ? y[4 + i] : y[i]);
                o[i][0] = min(pk2(v[0], v[1]), SENT - 1); o[i][1] = min(pk2(v[2], v[3]), SENT - 1);
            }
            const bool odd = l & 1;
#pragma unroll
            for (int i = 0; i < 4; i += 2) {
                const unsigned s0 = odd ? o[i][0] : o[i + 1][0], s1 = odd ? o[i][1] : o[i + 1][1];
                const unsigned r0 = __builtin_amdgcn_update_dpp(0, s0, 0xB1, 0xf, 0xf, false), r1 = __builtin_amdgcn_update_dpp(0, s1, 0xB1, 0xf, 0xf, false);
                const v4u d = odd ? v4u{r0, r1, o[i + 1][0], o[i + 1][1]} : v4u{o[i][0], o[i][1], r0, r1};
                wst16(rw, OFF_YT + (l & 1) * 512 + (l >> 1) * 16 + i * 512, ch * 4096 + mh * 2048, d);
            }
        }
        STAMP(8);
        if (w4 == 0) {
            __builtin_amdgcn_s_sleep(D3);
            const int lq = l & 7, co = l >> 3;
            const unsigned yo = OFF_YT + (64 * hp + 8 * co) * 4096 + (8 * c + lq) * 16;
            v4u yg[8] = {S4, S4, S4, S4, S4, S4, S4, S4};
            NOUNR for (int it = 0; it < SPIN_G; it++) { CBAR();
                unsigned m = 0;
#pragma unroll
                for (int e = 0; e < 8; e++) if (mx4(yg[e]) == SENT) yg[e] = wld16(rw, yo, e * 4096);
#pragma unroll
                for (int e = 0; e < 8; e++) m = max(m, mx4(yg[e]));
                if (__all(m != SENT)) break;
            }
            STAMP(9);
#pragma unroll
            for (int i = 0; i < 8; i++) {
                const unsigned sel = (i & 1) ? 0x07060302u : 0x05040100u;
                const v4u o = {__builtin_amdgcn_perm(yg[1][i >> 1], yg[0][i >> 1], sel), __builtin_amdgcn_perm(yg[3][i >> 1], yg[2][i >> 1], sel),
                               __builtin_amdgcn_perm(yg[5][i >> 1], yg[4][i >> 1], sel), __builtin_amdgcn_perm(yg[7][i >> 1], yg[6][i >> 1], sel)};
                st16<OUTAUX>(ro, (8 * lq + i) * 1792 + co * 16, 64 * c * 1792 + 896 + hp * 128, o);
            }
            STAMP(10);
        }
    }
}

extern "C" void kernel_launch(void* const* d_in, const int* in_sizes, int n_in,
                              void* d_out, int out_size, void* d_ws, size_t ws_size,
                              hipStream_t stream) {
    fused_kernel<<<256, 512, 0, stream>>>((const char*)d_in[0], (const char*)d_in[1], (const char*)d_in[2], (const char*)d_in[3],
                                          (const char*)d_in[4], (const char*)d_in[5], (const char*)d_in[6], (const char*)d_in[7],
                                          (const char*)d_in[8], (const char*)d_in[9], (char*)d_out, (char*)d_ws);
}
```

```cpp
#include <hip/hip_runtime.h>
#include <stdint.h>
#pragma clang diagnostic ignored "-Wint-to-pointer-cast"

typedef unsigned v4u __attribute__((ext_vector_type(4)));
typedef unsigned v2u __attribute__((ext_vector_type(2)));
typedef float v16f __attribute__((ext_vector_type(16)));
typedef float v4f __attribute__((ext_vector_type(4)));
typedef float v2f __attribute__((ext_vector_type(2)));
typedef __bf16 v8b __attribute__((ext_vector_type(8)));
typedef __bf16 v2b __attribute__((ext_vector_type(2)));
typedef short v4s __attribute__((ext_vector_type(4)));
#define LAS __attribute__((address_space(3)))
#define DI __device__ __forceinline__
typedef __amdgpu_buffer_rsrc_t rsrc_t;

constexpr unsigned OFF_SF = 4096;
constexpr unsigned OFF_PF = 16384;
constexpr unsigned OFF_UF = 32768;
constexpr unsigned OFF_QF = 65536;
constexpr unsigned OFF_PART = 1u << 20;
constexpr unsigned OFF_KV = 2u << 20;
constexpr unsigned OFF_YT = (2u << 20) + 262144;
constexpr unsigned OFF_WP = 4u << 20;
constexpr unsigned OFF_PQ = 5u << 20;
constexpr unsigned OFF_UT = 7u << 20;
constexpr unsigned WS_USED = 9u << 20;
constexpr unsigned L_K = 0, KSTR = 4512, L_U = 36096, USTR = 4224, L_T = 44544, L_S = L_T + 2704, L_X = 47616, L_W = L_X + 58368, L_END = L_W + 57344;
constexpr unsigned S_X = L_S, S_PF = L_S + 8, S_G = L_S + 16, S_V = L_S + 32, S_P = L_S + 40, S_WO = L_S + 48, S_PUB = L_S + 52, S_O = L_S + 64, S_C = L_S + 80,
                   S_TAG = L_S + 96, S_Q = L_S + 112, S_RD = L_S + 128;
constexpr unsigned OSTR = 912;
constexpr unsigned SENT = 0xFFFFFFFFu, TAGC = 0x9E3779B1u;
#define D1 10
#define D2 10
#define D3 10
#define OUTAUX 16
constexpr int SPIN_L = 1 << 20, SPIN_G = 1 << 14;
constexpr int LAG = 8;

#define STAMP(i)

DI float bflo(unsigned v) { return __uint_as_float(v << 16); }
DI float bfhi(unsigned v) { return __uint_as_float(v & 0xffff0000u); }
DI unsigned pk2(float a, float b) { v2f f = {a, b}; return __builtin_bit_cast(unsigned, __builtin_convertvector(f, v2b)); }
DI float rbf(float a) { return bflo(pk2(a, a)); }
DI v16f mfma32(v4u a, v4u b, v16f c) { return __builtin_amdgcn_mfma_f32_32x32x16_bf16(__builtin_bit_cast(v8b, a), __builtin_bit_cast(v8b, b), c, 0, 0, 0); }
DI v4f mfma16(v4u a, v4u b, v4f c) { return __builtin_amdgcn_mfma_f32_16x16x32_bf16(__builtin_bit_cast(v8b, a), __builtin_bit_cast(v8b, b), c, 0, 0, 0); }
DI v4u lr16(unsigned a) { return *(const LAS v4u*)a; }
DI v2u lr8(unsigned a) { return *(const LAS v2u*)a; }
DI unsigned lr4(unsigned a) { return *(const LAS unsigned*)a; }
DI void lw16(unsigned a, v4u v) { *(LAS v4u*)a = v; }
DI void lw8(unsigned a, v2u v) { *(LAS v2u*)a = v; }
DI void lw2(unsigned a, unsigned v) { *(LAS unsigned short*)a = (unsigned short)v; }
DI v2u ltr(unsigned a) { return __builtin_bit_cast(v2u, __builtin_amdgcn_ds_read_tr16_b64_v4i16((LAS v4s*)a)); }
DI v4u ltr2(unsigned a, unsigned b) { v2u x = ltr(a), y = ltr(b); return v4u{x.x, x.y, y.x, y.y}; }
DI void glds(const void* g, unsigned voff, unsigned lds) {
    asm volatile("s_mov_b32 m0, %2\n\ts_nop 0\n\tglobal_load_lds_dwordx4 %0, %1" :: "v"(voff), "s"(g), "s"(lds) : "memory");
}
#define VMWAIT(n) asm volatile("s_waitcnt vmcnt(" #n ")" ::: "memory")
#define CBAR() asm volatile("" ::: "memory")
#define SB() __builtin_amdgcn_sched_barrier(0)
#define PIN(x) asm volatile("" : "+v"(x))
#define NOUNR _Pragma("clang loop unroll(disable)")
#define WGBAR() asm volatile("s_waitcnt lgkmcnt(0)\n\ts_barrier" ::: "memory")
DI v4u wld16(rsrc_t r, unsigned v, unsigned s) { return __builtin_amdgcn_raw_buffer_load_b128(r, v, s, 16); }
DI v2u wld8(rsrc_t r, unsigned v, unsigned s) { return __builtin_amdgcn_raw_buffer_load_b64(r, v, s, 16); }
DI unsigned wld4(rsrc_t r, unsigned v, unsigned s) { return __builtin_amdgcn_raw_buffer_load_b32(r, v, s, 16); }
template <int AUX> DI void st16(rsrc_t r, unsigned v, unsigned s, v4u d) { __builtin_amdgcn_raw_buffer_store_b128(d, r, v, s, AUX); asm volatile("s_nop 1" :: "v"(d) : "memory"); }
DI void wst16(rsrc_t r, unsigned v, unsigned s, v4u d) { st16<16>(r, v, s, d); }
DI void wst4(rsrc_t r, unsigned v, unsigned s, unsigned d) { __builtin_amdgcn_raw_buffer_store_b32(d, r, v, s, 16); }
DI void lset(unsigned a, unsigned v) { CBAR(); *(volatile LAS unsigned*)a = v; CBAR(); }
DI void lwait1(unsigned a, unsigned v) {
    for (int i = 0; i < SPIN_L; i++) { unsigned x = *(volatile LAS unsigned*)a; if (__builtin_amdgcn_readfirstlane(x) >= v) break; }
    CBAR();
}
DI void lwait2(unsigned a, unsigned v) {
    for (int i = 0; i < SPIN_L; i++) { v2u x = *(volatile LAS v2u*)a; if (__builtin_amdgcn_readfirstlane(min(x.x, x.y)) >= v) break; }
    CBAR();
}
DI void lwait4(unsigned a, unsigned v) {
    for (int i = 0; i < SPIN_L; i++) { v4u x = *(volatile LAS v4u*)a; if (__builtin_amdgcn_readfirstlane(min(min(x.x, x.y), min(x.z, x.w))) >= v) break; }
    CBAR();
}
DI unsigned get_tag() {
    v2u x;
    for (int i = 0; i < SPIN_L; i++) { x = *(volatile LAS v2u*)S_TAG; if (__builtin_amdgcn_readfirstlane(x.x) != 0) break; }
    CBAR();
    return __builtin_amdgcn_readfirstlane(x.y);
}
DI unsigned tslice() { return ((unsigned)__builtin_amdgcn_s_memrealtime() >> 4) & 15; }
DI void poll_ts(rsrc_t rw, unsigned vo, unsigned stride, unsigned tag) {
    NOUNR for (int it = 0; it < SPIN_G; it++) { CBAR(); unsigned f = wld4(rw, vo, tslice() * stride); if (__all(f == tag)) break; }
    CBAR();
}
DI void wait_sf(rsrc_t rw, v2u f, int l, unsigned tag) {
    NOUNR for (int it = 0; it < SPIN_G; it++) { if (__all(f.x == tag && f.y == tag)) break; CBAR(); f = wld8(rw, OFF_SF + l * 8, 0); }
    CBAR();
}
DI unsigned mx4(v4u a) { return max(max(a.x, a.y), max(a.z, a.w)); }
DI v4u nosent(v4u a) { return v4u{min(a.x, SENT - 1), min(a.y, SENT - 1), min(a.z, SENT - 1), min(a.w, SENT - 1)}; }
constexpr float TINIT = 1.000001f, LOG2E = 1.44269504088896f;
DI float phiT(float T) { return fmaxf(T, __builtin_amdgcn_fmed3f(__builtin_amdgcn_exp2f(__builtin_fmaf(T, LOG2E, -TINIT * LOG2E)), 0.f, 1.f) + 1e-6f); }
DI v4u unit_sw(unsigned a0, unsigned a1, unsigned b0, unsigned b1) {
    auto r0 = __builtin_amdgcn_permlane32_swap(a0, b0, false, false);
    auto r1 = __builtin_amdgcn_permlane32_swap(a1, b1, false, false);
    return v4u{r0[0], r1[0], r0[1], r1[1]};
}
DI v2f cmul(v2f a, v2f b) { v2f t = v2f{a.x, a.x} * b; v2f bs = {-b.y, b.x}; return __builtin_elementwise_fma(v2f{a.y, a.y}, bs, t); }
DI v2f zpow(float a2, float th, float p) {
    float m = __builtin_amdgcn_exp2f(a2 * p), r = __builtin_amdgcn_fractf(th * p);
    return v2f{m * __builtin_amdgcn_cosf(r), m * __builtin_amdgcn_sinf(r)};
}
DI v16f splat16(float v) { return v16f{v, v, v, v, v, v, v, v, v, v, v, v, v, v, v, v}; }
template <int R0> DI void wrows4(unsigned base, const unsigned* a, const unsigned* b) {
    asm volatile("s_mov_b32 m0, %8\n\ts_nop 0\n\t"
                 "ds_write_addtid_b32 %0 offset:%9\n\tds_write_addtid_b32 %4 offset:%9+256\n\t"
                 "ds_write_addtid_b32 %1 offset:%9+528\n\tds_write_addtid_b32 %5 offset:%9+784\n\t"
                 "ds_write_addtid_b32 %2 offset:%9+1056\n\tds_write_addtid_b32 %6 offset:%9+1312\n\t"
                 "ds_write_addtid_b32 %3 offset:%9+1584\n\tds_write_addtid_b32 %7 offset:%9+1840"
                 :: "v"(a[0]), "v"(a[1]), "v"(a[2]), "v"(a[3]), "v"(b[0]), "v"(b[1]), "v"(b[2]), "v"(b[3]), "s"(base), "i"(R0 * 528) : "memory");
}

__global__ void __launch_bounds__(512, 2)
fused_kernel(const char* __restrict__ x, const char* __restrict__ wqkv, const char* __restrict__ wo, const char* __restrict__ u,
             const char* __restrict__ lar, const char* __restrict__ aim, const char* __restrict__ cre, const char* __restrict__ cim,
             const char* __restrict__ ldt, const char* __restrict__ dsk, char* __restrict__ out, char* ws) {
    __shared__ __attribute__((aligned(16))) char smem[L_END];
    STAMP(0);
    const unsigned long long T0 = __builtin_amdgcn_s_memrealtime();
    asm volatile("" :: "s"(x), "s"(wqkv), "s"(wo), "s"(u), "s"(lar), "s"(aim), "s"(cre), "s"(cim), "s"(ldt), "s"(dsk), "s"(out), "s"(ws));
    const int b = blockIdx.x, tid = threadIdx.x, l = tid & 63;
    const int w = __builtin_amdgcn_readfirstlane(tid >> 6);
    const int xcd = b & 7, slot = b >> 3;
    const int hp = 4 * (xcd & 1) + 3 - (slot >> 3), c = 8 * (xcd >> 1) + (slot & 7);
    const int lid = hp * 32 + c;
    const bool mainb = hp < 7;
    const int r32 = l & 31, half = l >> 5;
    const rsrc_t rw = __builtin_amdgcn_make_buffer_rsrc(ws, 0, WS_USED, 0x00020000);
    const rsrc_t ro = __builtin_amdgcn_make_buffer_rsrc(out, 0, 2048 * 1792, 0x00020000);
    if ((unsigned)(uintptr_t)smem != 0) return;
    const v4u S4 = {SENT, SENT, SENT, SENT};

    if (w < 4) {
        const int hl = w & 1, isV = w < 2 ? 1 : 0, h = 2 * hp + hl;
        unsigned tag;
        if (w < 2) {
            const unsigned vo = (l >> 3) * 896 + (((l & 7) ^ (4 * w + (l >> 4))) << 4);
            const unsigned vo2 = (l >> 3) * 2688 + (((l & 7) ^ (((l >> 4) & 1) << 2)) << 4);
            const char* gx = x + (64 * c + 8 * w) * 896;
            const char* gw = wqkv + min(hp, 6) * 128 + w * 8 * 2688;
#pragma unroll
            for (int kc = 0; kc < 7; kc++) {
#pragma unroll
                for (int rg = 0; rg < 4; rg++) glds(gx + rg * 16 * 896 + kc * 128, vo, L_X + kc * 8192 + (2 * rg + w) * 1024);
                if (mainb) {
#pragma unroll
                    for (int i = 4 * kc; i < 4 * kc + 4; i++) glds(gw + i * 16 * 2688, vo2, L_W + (2 * i + w) * 1024);
                }
                if (kc == 0) WGBAR();
                if (kc == 5) { if (mainb) VMWAIT(16); else VMWAIT(8); lset(S_X + 4 * w, 1); }
            }
            lset(S_RD + 4 * w, 1);
            tag = get_tag();
            STAMP(1);
        } else if (w == 2) {
            const int lq = l & 7, co = l >> 3;
            v4u ut[8];
            const char* gu = u + (64 * c + 8 * lq) * 896 + min(hp, 6) * 128 + co * 16;
#pragma unroll
            for (int i = 0; i < 8; i++) ut[i] = *(const v4u*)(gu + i * 896);
            CBAR();
            const unsigned E = *(const __attribute__((address_space(4))) unsigned*)ws;
            WGBAR();
            tag = E + TAGC;
            *(volatile LAS v2u*)S_TAG = v2u{1u, tag};
            STAMP(1);
            const unsigned uo = mainb ? OFF_UT + (64 * hp + 8 * co) * 4096 + (8 * c + lq) * 16 : 0x80000000u;
#pragma unroll
            for (int e = 0; e < 8; e++) {
                const unsigned sel = (e & 1) ? 0x07060302u : 0x05040100u;
                wst16(rw, uo, e * 4096, v4u{__builtin_amdgcn_perm(ut[6][e >> 1], ut[7][e >> 1], sel), __builtin_amdgcn_perm(ut[4][e >> 1], ut[5][e >> 1], sel),
                                            __builtin_amdgcn_perm(ut[2][e >> 1], ut[3][e >> 1], sel), __builtin_amdgcn_perm(ut[0][e >> 1], ut[1][e >> 1], sel)});
            }
            CBAR();
            STAMP(2);
        } else {
            const int ph = lid / 28, pk = lid - 28 * ph;
            const unsigned dvo = r32 < 28 ? OFF_WP + half * 448 + r32 * 16 : 0x80000000u;
            const unsigned dso = 2 * ph * 25088 + pk * 896;
            if (mainb) {
                const char* gw = wqkv + 16 * pk * 2688 + 896 + ph * 112;
#pragma unroll
                for (int i = 0; i < 4; i++) { const int q = min(64 * i + l, 223), r = q / 14, p = q - 14 * r; glds(gw, r * 2688 + p * 16 + (p >= 7 ? 784 : 0), L_U + i * 1024); }
#pragma unroll
                for (int uu = 0; uu < 4; uu++) wst16(rw, dvo, dso + (uu >> 1) * 16 * 25088 + (uu & 1) * 25088, S4);
            }
            WGBAR();
            tag = get_tag();
            STAMP(1);
            if (mainb) {
                VMWAIT(0);
                if (l < 16) wst4(rw, OFF_PF + l * 1024 + ph * 128 + pk * 4, 0, tag);
                const unsigned ta = L_U + (8 * half + ((l >> 2) & 3)) * 224 + ((l >> 4) & 1) * 32 + (l & 3) * 8;
                v4u f[4];
#pragma unroll
                for (int uu = 0; uu < 4; uu++) f[uu] = ltr2(ta + uu * 56, ta + uu * 56 + 4 * 224);
#pragma unroll
                for (int uu = 0; uu < 4; uu++) wst16(rw, dvo, dso + (uu >> 1) * 16 * 25088 + (uu & 1) * 25088, nosent(f[uu]));
            }
            STAMP(2);
            poll_ts(rw, OFF_PF + hp * 128 + min(r32, 27) * 4, 1024, tag);
            lset(S_PF, 1);
        }
        lwait1(S_PF, 1);
        lwait2(S_RD, 1);
        STAMP(3);
        const int shift = (!isV && hl) ? 4 : 0;
        const unsigned wvo = OFF_WP + (isV * 16 + h) * 25088 + half * 448 + min(max(r32 - shift, 0), 27) * 16;
        v4u wf[28];
#pragma unroll
        for (int ks = 0; ks < 28; ks++) wf[ks] = wld16(rw, wvo, ks * 896);
        v2u sf = wld8(rw, OFF_SF + l * 8, 0);
        CBAR();
        unsigned xa[4];
#pragma unroll
        for (int m = 0; m < 4; m++) xa[m] = L_X + r32 * 128 + (((2 * m + half) ^ ((l >> 1) & 7)) << 4);
        const int tq = (l >> 2) & 3, tp = l & 3, c16 = (l >> 4) & 1;
        const unsigned trc = L_W + tq * 128 + (((4 * hl + 2 * c16 + (tp >> 1)) ^ ((tq >> 1) << 2)) << 4) + 8 * (tp & 1);
        const unsigned trb = trc + half * 1024;
        const int rh = w >> 1;
        lwait2(S_X, 1);
        STAMP(4);
        v16f q = splat16(TINIT);
        {
            unsigned xq[4];
#pragma unroll
            for (int m = 0; m < 4; m++) xq[m] = xa[m] + rh * 4096;
            v4u fa[28], fb[28];
            if (mainb) {
#pragma unroll
                for (int ks = 0; ks < 16; ks++) {
                    fa[ks] = ltr2(trb + ks * 2048, trb + ks * 2048 + 512); fb[ks] = lr16(xq[ks & 3] + (ks >> 2) * 8192);
                    if (ks >= 3) q = mfma32(fa[ks - 3], fb[ks - 3], q);
                    SB();
                }
            }
            VMWAIT(29);
            if (w < 2) lset(S_X + 4 * w, 2);
            else if (w == 2 && l < 16) wst4(rw, OFF_UF + l * 1024 + lid * 4, 0, tag);
            lwait2(S_X, 2);
            if (mainb) {
#pragma unroll
                for (int ks = 16; ks < 31; ks++) {
                    if (ks < 28) { fa[ks] = ltr2(trb + ks * 2048, trb + ks * 2048 + 512); fb[ks] = lr16(xq[ks & 3] + (ks >> 2) * 8192); }
                    q = mfma32(fa[ks - 3], fb[ks - 3], q);
                    SB();
                }
            }
        }
        STAMP(5);
        const unsigned pqo = mainb ? OFF_PQ + l * 16 : 0x80000000u;
        const float ini = isV ? 0.f : TINIT;
        v16f a0, a1;
        v4u p1[28];
        NOUNR for (int it = 0; it < SPIN_G; it++) { CBAR();
            a0 = splat16(ini); a1 = splat16(ini);
            asm volatile("" : "+v"(q));
            unsigned m = 0;
            v4u p0[28];
            unsigned qp[8];
            p0[0] = lr16(xa[0]); p0[1] = lr16(xa[1]);
            SB();
#pragma unroll
            for (int ks = 0; ks < 28; ks++) {
                if (ks + 2 < 28) p0[ks + 2] = lr16(xa[(ks + 2) & 3] + ((ks + 2) >> 2) * 8192);
                if (ks + 2 >= LAG) p1[ks + 2 - LAG] = lr16(xa[(ks + 2 - LAG) & 3] + 4096 + ((ks + 2 - LAG) >> 2) * 8192);
                a0 = mfma32(p0[ks], wf[ks], a0);
                if (ks >= LAG) a1 = mfma32(p1[ks - LAG], wf[ks - LAG], a1);
                m = max(m, mx4(wf[ks]));
                if (ks >= LAG && ks < LAG + 8) qp[ks - LAG] = pk2(phiT(q[2 * (ks - LAG)]), phiT(q[2 * (ks - LAG) + 1]));
                if (ks == LAG + 4 || ks == LAG + 8) {
                    const int s = ks == LAG + 8;
                    wst16(rw, pqo, (2 * c + rh) * 28672 + (8 * hp + 4 * hl + 2 * s) * 512, unit_sw(qp[4 * s], qp[4 * s + 1], qp[4 * s + 2], qp[4 * s + 3]));
                }
                SB();
            }
            if (__all(m != SENT)) break;
#pragma unroll
            for (int ks = 0; ks < 28; ks++) wf[ks] = wld16(rw, wvo, ks * 896);
        }
        lset(S_G + 4 * w, 1);
        STAMP(6);
        if (isV) {
            unsigned vf[16];
#pragma unroll
            for (int j = 0; j < LAG; j++) {
                const int ks = 28 - LAG + j;
                if (ks + 2 < 28) p1[ks + 2] = lr16(xa[(ks + 2) & 3] + 4096 + ((ks + 2) >> 2) * 8192);
                a1 = mfma32(p1[ks], wf[ks], a1);
                vf[j] = r32 == 28 ? 0x3F803F80u : pk2(a0[2 * j], a0[2 * j + 1]);
                PIN(vf[j]);
                SB();
            }
            wait_sf(rw, sf, l, tag);
            lwait4(S_G, 1);
#pragma unroll
            for (int j = 0; j < 8; j++) vf[8 + j] = r32 == 28 ? 0x3F803F80u : pk2(a1[2 * j], a1[2 * j + 1]);
#pragma unroll
            for (int s = 0; s < 4; s++) lw16(L_X + hl * 4096 + s * 1024 + l * 16, v4u{vf[4 * s], vf[4 * s + 1], vf[4 * s + 2], vf[4 * s + 3]});
            lset(S_V + 4 * hl, 1);
            VMWAIT(0);
            lset(S_Q + 4 * w, 1);
            if (w == 1 && mainb) {
                const unsigned vo2 = (l >> 3) * 896 + (((l & 7) ^ (((l >> 4) & 1) << 2)) << 4);
                const char* gw = wo + hp * 128;
#pragma unroll
                for (int i = 0; i < 56; i++) {
                    glds(gw + i * 8 * 896, vo2, L_W + i * 1024);
                    if (i == 15) { lwait4(S_Q, 1); if (l < 16) wst4(rw, OFF_QF + l * 4096 + c * 128 + hp * 4, 0, tag); }
                }
                VMWAIT(0);
                lset(S_WO, 1);
            }
        } else {
            unsigned kf[16];
#pragma unroll
            for (int j = 0; j < LAG; j++) {
                const int ks = 28 - LAG + j;
                if (ks + 2 < 28) p1[ks + 2] = lr16(xa[(ks + 2) & 3] + 4096 + ((ks + 2) >> 2) * 8192);
                a1 = mfma32(p1[ks], wf[ks], a1);
                kf[j] = pk2(phiT(a0[2 * j]), phiT(a0[2 * j + 1]));
                PIN(kf[j]);
                SB();
            }
            VMWAIT(0);
            lset(S_Q + 4 * w, 1);
#pragma unroll
            for (int j = 0; j < 8; j++) { kf[8 + j] = pk2(phiT(a1[2 * j]), phiT(a1[2 * j + 1])); PIN(kf[8 + j]); }
            lwait1(S_V + 4 * hl, 1);
            v4u vv[4];
#pragma unroll
            for (int s = 0; s < 4; s++) vv[s] = lr16(L_X + hl * 4096 + s * 1024 + l * 16);
            v16f d = splat16(0.f);
#pragma unroll
            for (int s = 0; s < 4; s++) d = mfma32(v4u{kf[4 * s], kf[4 * s + 1], kf[4 * s + 2], kf[4 * s + 3]}, vv[s], d);
            const unsigned mo = (hl && !half) ? 0u : ~0u, me = (!hl && half) ? 0u : ~0u;
            const unsigned po = OFF_PART + ((4 * h + half) * 4 + (r32 >> 3)) * 4096 + c * 128 + (l & 7) * 16;
            wst16(rw, po, 0, nosent(unit_sw(pk2(d[0], d[1]) & mo, pk2(d[2], d[3]) & mo, pk2(d[4], d[5]), pk2(d[6], d[7]))));
            wst16(rw, po, 8 * 4096, nosent(unit_sw(pk2(d[8], d[9]), pk2(d[10], d[11]), pk2(d[12], d[13]) & me, pk2(d[14], d[15]) & me)));
            lset(S_P + 4 * hl, 1);
        }
        STAMP(7);
        if (w == 0) {
            lwait2(S_P, 1);
            __builtin_amdgcn_s_sleep(D1);
            v4u a[4] = {S4, S4, S4, S4};
            NOUNR for (int it = 0; it < SPIN_G; it++) { CBAR();
                unsigned m = 0;
#pragma unroll
                for (int j = 0; j < 4; j++) if (mx4(a[j]) == SENT) a[j] = wld16(rw, OFF_PART + l * 16 + j * 1024, lid * 4096);
                lset(S_RD, 2);
#pragma unroll
                for (int j = 0; j < 4; j++) m = max(m, mx4(a[j]));
                if (__all(m != SENT)) break;
            }
            STAMP(8);
            float s[8];
#pragma unroll
            for (int i = 0; i < 4; i++) {
                s[2 * i] = (bflo(a[0][i]) + bflo(a[1][i])) + (bflo(a[2][i]) + bflo(a[3][i]));
                s[2 * i + 1] = (bfhi(a[0][i]) + bfhi(a[1][i])) + (bfhi(a[2][i]) + bfhi(a[3][i]));
            }
            float t[4], v[2];
#pragma unroll
            for (int i = 0; i < 4; i++) { auto r = __builtin_amdgcn_permlane32_swap(__float_as_uint(s[i]), __float_as_uint(s[i + 4]), false, false); t[i] = __uint_as_float(r[0]) + __uint_as_float(r[1]); }
#pragma unroll
            for (int i = 0; i < 2; i++) { auto r = __builtin_amdgcn_permlane16_swap(__float_as_uint(t[i]), __float_as_uint(t[i + 2]), false, false); v[i] = __uint_as_float(r[0]) + __uint_as_float(r[1]); }
#pragma unroll
            for (int i = 0; i < 2; i++) v[i] += __uint_as_float(__builtin_amdgcn_update_dpp(0, __float_as_uint(v[i]), 0x128, 0xf, 0xf, false));
            const unsigned pv = min(pk2(v[0], v[1]), SENT - 1);
            const unsigned ko = OFF_KV + ((l >> 3) & 1) * 32768 + lid * 128 + (l & 7) * 16 + half * 8 + ((l >> 4) & 1) * 4;
#pragma unroll
            for (int i = 0; i < 4; i++) wst4(rw, ko, i * 65536, pv);
            lset(S_PUB, 1);
            STAMP(9);
        }
        if (mainb) {
            poll_ts(rw, OFF_QF + c * 128 + min(l, 6) * 4, 4096, tag);
            if (lid == 0) poll_ts(rw, OFF_UF + w * 256 + l * 4, 1024, tag);
            lwait1(S_RD, 2);
            v4u pq[16];
#pragma unroll
            for (int i = 0; i < 16; i++) {
                const int pp = i >> 3, hh = (i >> 2) & 1, s = (i >> 1) & 1, rt = i & 1;
                pq[i] = wld16(rw, OFF_PQ + l * 16 + (7 * pp + 3 * hh + 2 * s) * 512, (2 * c + rt) * 28672 + 14 * w * 512);
            }
            CBAR();
            STAMP(10);
            lwait1(S_WO, 1);
            v4u of[28];
            {
                const unsigned t4 = trc + half * 512, t24 = trc + half * 24 * 128, t56 = trc + half * 56 * 128;
#pragma unroll
                for (int g = 0; g < 4; g++) {
                    const unsigned o = g * 112 * 128;
#pragma unroll
                    for (int p = 0; p < 2; p++) {
                        const unsigned op = o + p * 56 * 128;
                        of[7 * g + 3 * p + 0] = ltr2(t4 + op, t4 + op + 8 * 128);
                        of[7 * g + 3 * p + 1] = ltr2(t4 + op + 28 * 128, t4 + op + 36 * 128);
                        of[7 * g + 3 * p + 2] = ltr2(t4 + op + 16 * 128, t24 + op + 24 * 128);
                    }
                    of[7 * g + 6] = ltr2(t56 + o + 44 * 128, t56 + o + 52 * 128);
                }
            }
            lwait1(S_PUB, 1);
            __builtin_amdgcn_s_sleep(D2);
            STAMP(11);
            v4u kv[8] = {S4, S4, S4, S4, S4, S4, S4, S4};
            unsigned so = w * 8192;
            NOUNR for (int it = 0; it < SPIN_G; it++) { CBAR();
                unsigned m = 0;
#pragma unroll
                for (int j = 0; j < 8; j++) if (mx4(kv[j]) == SENT) kv[j] = wld16(rw, OFF_KV + l * 16 + j * 1024, so);
#pragma unroll
                for (int j = 0; j < 8; j++) m = max(m, mx4(kv[j]));
                if (__all(m != SENT)) break;
                so = (so + 32768) & 262143;
            }
            STAMP(12);
            {
                v16f n[8];
                unsigned keep[4];
                const unsigned oa = L_X + r32 * OSTR + w * 224 + half * 16;
                const unsigned ya0 = L_X + r32 * OSTR + (half ? 896 : w * 224 + 192), ya1 = L_X + r32 * OSTR + (half ? 896 : w * 224 + 208);
#pragma unroll
                for (int j = 0; j < 9; j++) {
                    if (j < 8) {
                        const int rt = j >> 2, hh = j & 3;
                        n[j] = mfma32(kv[2 * hh], pq[4 * hh + rt], splat16(0.f));
                        n[j] = mfma32(kv[2 * hh + 1], pq[4 * hh + 2 + rt], n[j]);
                    }
                    if (j > 0) {
                        const int rt = (j - 1) >> 2, hh = (j - 1) & 3;
                        const v16f a = n[j - 1];
                        auto r = __builtin_amdgcn_permlane32_swap(__float_as_uint(a[12]), __float_as_uint(a[12]), false, false);
                        const float rc = __builtin_amdgcn_rcpf(__uint_as_float(r[1]) + 1e-6f);
                        unsigned o[8];
#pragma unroll
                        for (int i = 0; i < 8; i++) o[i] = pk2(a[2 * i] * rc, a[2 * i + 1] * rc);
                        const unsigned ob = oa + rt * 32 * OSTR + (hh >> 1) * 96;
                        lw16(ob + (hh & 1) * 32, v4u{o[0], o[1], o[2], o[3]});
                        if (hh & 1) {
                            lw16(ob + 64, v4u{keep[0], keep[1], half ? o[4] : keep[2], half ? o[5] : keep[3]});
                            lw16((hh & 2 ? ya1 : ya0) + rt * 32 * OSTR, v4u{o[4], o[5], o[6], o[7]});
                        } else {
#pragma unroll
                            for (int i = 0; i < 4; i++) keep[i] = o[4 + i];
                        }
                    }
                    SB();
                }
            }
            lset(S_O + 4 * w, 1);
            lwait4(S_O, 1);
            STAMP(13);
            if (lid == 0 && w == 0 && l == 0) wst4(rw, 0, 0, tag - TAGC + 1);
            v16f acc = splat16(0.f);
            {
                const unsigned ob = L_X + (32 * rh + r32) * OSTR + half * 16;
                v4u fo[28];
#pragma unroll
                for (int ks = 0; ks < 32; ks++) {
                    if (ks < 28) fo[ks] = lr16(ob + ks * 32);
                    if (ks >= 4) acc = mfma32(of[ks - 4], fo[ks - 4], acc);
                    SB();
                }
            }
#pragma unroll
            for (int s = 0; s < 2; s++)
                st16<OUTAUX>(ro, (32 * rh + r32) * 1792 + hl * 64 + s * 32 + half * 16, 64 * c * 1792 + hp * 128,
                             unit_sw(pk2(acc[8 * s], acc[8 * s + 1]), pk2(acc[8 * s + 2], acc[8 * s + 3]), pk2(acc[8 * s + 4], acc[8 * s + 5]), pk2(acc[8 * s + 6], acc[8 * s + 7])));
            STAMP(14);
        }
    } else if (!mainb) {
        const int sw = 4 * c + w - 4;
#pragma unroll
        for (int i = 0; i < 24; i++) { wst16(rw, OFF_PART + l * 16 + i * 1024, sw * 24576, S4); if (i == 3) { if (w == 7 && l < 16) lw16(L_T + 2688 + l * 16, v4u{0u, 0u, 0u, 0u}); WGBAR(); } }
        const unsigned tag = get_tag();
        STAMP(1);
        VMWAIT(0);
        if (l == 0) wst4(rw, OFF_SF + sw * 4, 0, tag);
        STAMP(2);
    } else {
        const int w4 = w - 4, chl = w4 & 1, mh = w4 >> 1, ch = 2 * lid + chl;
        const unsigned po = ch * 512 + mh * 256 + l * 4;
        const unsigned p_ar = *(const unsigned*)(lar + po), p_ai = *(const unsigned*)(aim + po), p_cr = *(const unsigned*)(cre + po), p_ci = *(const unsigned*)(cim + po);
        const unsigned p_dt = *(const unsigned short*)(ldt + ch * 2), p_d = *(const unsigned short*)(dsk + ch * 2);
        CBAR();
        if (w == 7 && l < 16) lw16(L_T + 2688 + l * 16, v4u{0u, 0u, 0u, 0u});
        WGBAR();
        unsigned ga[11];
#pragma unroll
        for (int s = 0; s < 11; s++) {
            const int ll = 8 * (64 * w4 + l) - 3 + s;
            const int code = (int)(pk2((float)ll, 0.f) & 0xffffu);
            const int idx = ll < 0 ? 672 : max(code - 0x4280, min(ll, 128));
            ga[s] = L_T + idx * 4;
        }
        const int g = l >> 4, lov = (l & 15) + 16 * mh;
        const unsigned tw0 = L_T + chl * 2 + 4 * (g < 2 ? 4 * g + 8 * lov : (g == 2 ? 256 + 4 * lov : 384 + 2 * lov));
        const unsigned tw0b = tw0 + (g == 3 ? 248 : 0);
        const unsigned tw1 = L_T + chl * 2 + 4 * (g < 2 ? 512 + 128 * g + lov : 673), tw1b = g == 0 ? tw1 : L_T + 4 * 673 - 128;
#pragma unroll
        for (int s = 0; s < 11; s++) asm volatile("" : "+v"(ga[s]));
        const unsigned tag = get_tag();
        STAMP(1);
        {
            const float dt = rbf(__expf(bflo(p_dt)));
            v2f z1[2], z8[2], z256[2], cc[2];
#pragma unroll
            for (int m = 0; m < 2; m++) {
                const float ar = -rbf(__expf(m ? bfhi(p_ar) : bflo(p_ar))) * dt, ai = (m ? bfhi(p_ai) : bflo(p_ai)) * dt;
                const float a2 = ar * LOG2E, th = ai * 0.159154943091895f;
                z8[m] = zpow(a2, th, 8.f);
                z1[m] = zpow(a2, -th, 1.f);
                z256[m] = zpow(a2, -th, 256.f);
                cc[m] = v2f{(m ? bfhi(p_cr) : bflo(p_cr)) * dt * 2.f, (m ? bfhi(p_ci) : bflo(p_ci)) * dt * -2.f};
            }
            STAMP(2);
            const unsigned sb = L_K + w4 * 8448, sr = sb + (l & 15) * 528 + (l >> 4) * 16;
            v4u lo[2][8];
            v2f g0 = {1.f, 0.f}, g1 = {1.f, 0.f};
            unsigned pa[8], pb[8];
#define LOQ(R0, LAST) \
            for (int r = 0; r < 8; r++) { pa[r] = pk2(g0.x, g0.y); pb[r] = pk2(g1.x, g1.y); if (!(LAST && r == 7)) { g0 = cmul(g0, z8[0]); g1 = cmul(g1, z8[1]); } } \
            wrows4<R0>(sb, pa, pb); wrows4<R0 + 4>(sb, pa + 4, pb + 4);
#pragma unroll
            LOQ(0, false)
#pragma unroll
            LOQ(8, false)
#pragma unroll
            for (int ks = 0; ks < 8; ks++) lo[0][ks] = lr16(sr + ks * 64);
            SB();
#pragma unroll
            LOQ(0, false)
#pragma unroll
            LOQ(8, true)
#pragma unroll
            for (int ks = 0; ks < 8; ks++) lo[1][ks] = lr16(sr + ks * 64);
            SB();
            v2f hh[2][21];
            unsigned hq[2][24];
#pragma unroll
            for (int m = 0; m < 2; m++) {
                hh[m][0] = cc[m];
#pragma unroll
                for (int r = 1; r < 8; r++) hh[m][r] = cmul(hh[m][r - 1], z1[m]);
#pragma unroll
                for (int r = 8; r < 12; r++) hh[m][r] = cmul(hh[m][2 * (r - 8)], z256[m]);
                hh[m][12] = cmul(hh[m][8], z256[m]); hh[m][13] = cmul(hh[m][10], z256[m]);
                hh[m][14] = cmul(hh[m][12], z256[m]); hh[m][15] = cmul(hh[m][13], z256[m]);
#pragma unroll
                for (int r = 0; r < 16; r++) hq[m][r] = pk2(hh[m][r].x, hh[m][r].y);
            }
            wrows4<0>(sb, hq[0], hq[1]); wrows4<4>(sb, hq[0] + 4, hq[1] + 4); wrows4<8>(sb, hq[0] + 8, hq[1] + 8); wrows4<12>(sb, hq[0] + 12, hq[1] + 12);
            v4u fa[8];
#pragma unroll
            for (int ks = 0; ks < 8; ks++) fa[ks] = lr16(sr + ks * 64);
            SB();
#pragma unroll
            for (int m = 0; m < 2; m++) {
                hh[m][16] = cmul(hh[m][14], z256[m]);
#pragma unroll
                for (int r = 17; r < 21; r++) hh[m][r] = cmul(hh[m][r - 1], z256[m]);
#pragma unroll
                for (int r = 16; r < 24; r++) hq[m][r] = pk2(hh[m][min(r, 20)].x, hh[m][min(r, 20)].y);
            }
            wrows4<0>(sb, hq[0] + 16, hq[1] + 16); wrows4<4>(sb, hq[0] + 20, hq[1] + 20);
            v4u fb[8];
#pragma unroll
            for (int ks = 0; ks < 8; ks++) fb[ks] = lr16(sr + ks * 64);
            SB();
            v4f d[2][2];
#pragma unroll
            for (int i = 0; i < 4; i++) d[i >> 1][i & 1] = v4f{0, 0, 0, 0};
#pragma unroll
            for (int ks = 0; ks < 8; ks++) { d[0][0] = mfma16(fa[ks], lo[0][ks], d[0][0]); d[0][1] = mfma16(fa[ks], lo[1][ks], d[0][1]); }
#pragma unroll
            for (int ks = 0; ks < 8; ks++) { d[1][0] = mfma16(fb[ks], lo[0][ks], d[1][0]); d[1][1] = mfma16(fb[ks], lo[1][ks], d[1][1]); }
            lw16(sb + l * 16, __builtin_bit_cast(v4u, mh ? d[0][0] : d[0][1]));
            lw16(sb + 1024 + l * 16, __builtin_bit_cast(v4u, mh ? d[1][0] : d[1][1]));
            lset(S_C + 4 * w4, 1);
            STAMP(3);
            lwait1(S_C + 4 * (w4 ^ 2), 1);
            const unsigned pbase = L_K + (w4 ^ 2) * 8448 + l * 16;
            v4f e0 = __builtin_bit_cast(v4f, lr16(pbase)) + (mh ? d[0][1] : d[0][0]);
            v4f e1 = __builtin_bit_cast(v4f, lr16(pbase + 1024)) + (mh ? d[1][1] : d[1][0]);
            if (mh == 0 && l == 0) e0[0] += bflo(p_d);
            const unsigned k0 = pk2(e0[0], e0[1]), k1 = pk2(e0[2], e0[3]), k2 = pk2(e1[0], e1[1]), k3 = pk2(e1[2], e1[3]);
            lw2(tw0, k0); lw2(tw0 + 4, k0 >> 16); lw2(tw0b + 8, k1); lw2(tw0b + 12, k1 >> 16);
            lw2(tw1, k2); lw2(tw1b + 128, k2 >> 16); lw2(g == 0 ? tw1 + 256 : tw1b + 128, k3); lw2(g == 0 ? tw1 + 384 : tw1b + 128, k3 >> 16);
        }
        lset(S_C + 4 * w4, 2);
        poll_ts(rw, OFF_UF + (lid >> 5) * 128 + r32 * 4, 1024, tag);
        v4u ug[2];
#pragma unroll
        for (int i = 0; i < 2; i++) ug[i] = wld16(rw, OFF_UT + l * 16 + i * 1024, ch * 4096 + mh * 2048);
        v2u sf = wld8(rw, OFF_SF + l * 8, 0);
        STAMP(4);
        lwait4(S_C, 2);
        {
            unsigned t[11];
#pragma unroll
            for (int s = 0; s < 11; s++) t[s] = lr4(ga[s]);
            const unsigned ka = L_K + 384 + (64 * w4 + l) * 16;
#pragma unroll
            for (int cc = 0; cc < 2; cc++) {
                unsigned P[10];
#pragma unroll
                for (int s = 0; s < 10; s++) P[s] = __builtin_amdgcn_perm(t[s + 1], t[s], cc ? 0x07060302u : 0x05040100u);
#pragma unroll
                for (int r = 0; r < 4; r++) lw16(ka + (cc * 4 + r) * KSTR, v4u{P[r], P[r + 2], P[r + 4], P[r + 6]});
            }
            if (l < 48) lw16(L_K + (2 * w4 + l / 24) * KSTR + (l % 24) * 16, v4u{0u, 0u, 0u, 0u});
        }
        STAMP(5);
        lwait1(S_PF, 1);
#pragma unroll
        for (int i = 0; i < 2; i++) lw16(L_U + chl * USTR + 4080 - (128 * mh + 64 * i + l) * 16, ug[i]);
        if (mh == 0 && l < 8) lw16(L_U + chl * USTR + 4096 + l * 16, v4u{0u, 0u, 0u, 0u});
        lset(S_C + 4 * w4, 3);
        lwait4(S_C, 3);
        STAMP(6);
        v4f y[8];
#pragma unroll
        for (int t = 0; t < 8; t++) y[t] = v4f{0, 0, 0, 0};
        {
            const unsigned ab = L_K + (chl * 4 + (l & 3)) * KSTR + 128 * ((l >> 2) & 3) + 16 * (l >> 4) + 256 * mh;
            const unsigned bb = L_U + chl * USTR + 2 * (2076 - 224) - 3584 - 8 * (l & 15) + 16 * (l >> 4) + 256 * mh;
            unsigned bb8 = bb + 8;
            asm volatile("" : "+v"(bb8));
            v4u A[5][8];
            v2u Bp[5][8], Bq[5][8];
#pragma unroll
            for (int rho = -1; rho < 4; rho++) {
                int n = 0;
#pragma unroll
                for (int s = 0; s < 8; s++)
#pragma unroll
                    for (int a = 0; a <= s; a++) {
                        if (rho < 3 && n < 24) {
                            const int i = n / 3, k = n % 3, o = 64 * (rho + 1);
                            if (k == 0) A[rho + 2][i] = lr16(ab + 512 * i + o);
                            else if (k == 1) Bp[rho + 2][i] = lr8(bb + 512 * (7 - i) + o);
                            else Bq[rho + 2][i] = lr8(bb8 + 512 * (7 - i) + o);
                        }
                        n++;
                        if (rho >= 0) {
                            const int bq = s - a;
                            y[s] = mfma16(A[rho + 1][a], v4u{Bp[rho + 1][bq].x, Bp[rho + 1][bq].y, Bq[rho + 1][bq].x, Bq[rho + 1][bq].y}, y[s]);
                        }
                        SB();
                    }
            }
        }
        lset(S_C + 4 * w4, 4);
        STAMP(7);
        wait_sf(rw, sf, l, tag);
        lwait4(S_C, 4);
#pragma unroll
        for (int i = 0; i < 4; i++) lw16(L_K + w4 * 4096 + i * 1024 + l * 16, __builtin_bit_cast(v4u, mh ? y[i] : y[4 + i]));
        lset(S_C + 4 * w4, 5);
        lwait1(S_C + 4 * (w4 ^ 2), 5);
        {
            unsigned o[4][2];
#pragma unroll
            for (int i = 0; i < 4; i++) {
                v4f v = __builtin_bit_cast(v4f, lr16(L_K + (w4 ^ 2) * 4096 + i * 1024 + l * 16)) + (mh ? y[4 + i] : y[i]);
                o[i][0] = min(pk2(v[0], v[1]), SENT - 1); o[i][1] = min(pk2(v[2], v[3]), SENT - 1);
            }
            const bool odd = l & 1;
#pragma unroll
            for (int i = 0; i < 4; i += 2) {
                const unsigned s0 = odd ? o[i][0] : o[i + 1][0], s1 = odd ? o[i][1] : o[i + 1][1];
                const unsigned r0 = __builtin_amdgcn_update_dpp(0, s0, 0xB1, 0xf, 0xf, false), r1 = __builtin_amdgcn_update_dpp(0, s1, 0xB1, 0xf, 0xf, false);
                const v4u d = odd ? v4u{r0, r1, o[i + 1][0], o[i + 1][1]} : v4u{o[i][0], o[i][1], r0, r1};
                wst16(rw, OFF_YT + (l & 1) * 512 + (l >> 1) * 16 + i * 512, ch * 4096 + mh * 2048, d);
            }
        }
        STAMP(8);
        if (w4 == 0) {
            __builtin_amdgcn_s_sleep(D3);
            const int lq = l & 7, co = l >> 3;
            const unsigned yo = OFF_YT + (64 * hp + 8 * co) * 4096 + (8 * c + lq) * 16;
            v4u yg[8] = {S4, S4, S4, S4, S4, S4, S4, S4};
            NOUNR for (int it = 0; it < SPIN_G; it++) { CBAR();
                unsigned m = 0;
#pragma unroll
                for (int e = 0; e < 8; e++) if (mx4(yg[e]) == SENT) yg[e] = wld16(rw, yo, e * 4096);
#pragma unroll
                for (int e = 0; e < 8; e++) m = max(m, mx4(yg[e]));
                if (__all(m != SENT)) break;
            }
            STAMP(9);
#pragma unroll
            for (int i = 0; i < 8; i++) {
                const unsigned sel = (i & 1) ? 0x07060302u : 0x05040100u;
                const v4u o = {__builtin_amdgcn_perm(yg[1][i >> 1], yg[0][i >> 1], sel), __builtin_amdgcn_perm(yg[3][i >> 1], yg[2][i >> 1], sel),
                               __builtin_amdgcn_perm(yg[5][i >> 1], yg[4][i >> 1], sel), __builtin_amdgcn_perm(yg[7][i >> 1], yg[6][i >> 1], sel)};
                st16<OUTAUX>(ro, (8 * lq + i) * 1792 + co * 16, 64 * c * 1792 + 896 + hp * 128, o);
            }
            STAMP(10);
        }
    }
    VMWAIT(0);
    if (threadIdx.x == 0) __hip_atomic_store((unsigned long long*)(ws + (100u << 20)) + blockIdx.x, T0, __ATOMIC_RELAXED, __HIP_MEMORY_SCOPE_AGENT);
}

__global__ void tiny1(char* ws) { if (threadIdx.x == 0) __hip_atomic_store((unsigned long long*)(ws + (101u << 20)), __builtin_amdgcn_s_memrealtime(), __ATOMIC_RELAXED, __HIP_MEMORY_SCOPE_AGENT); }
__global__ void tiny2(char* ws) {
    unsigned long long TM = ~0ull;
    for (int i = 0; i < 256; i++) { unsigned long long v = __hip_atomic_load((unsigned long long*)(ws + (100u << 20)) + i, __ATOMIC_RELAXED, __HIP_MEMORY_SCOPE_AGENT); TM = v < TM ? v : TM; }
    const unsigned long long TT = __hip_atomic_load((unsigned long long*)(ws + (101u << 20)), __ATOMIC_RELAXED, __HIP_MEMORY_SCOPE_AGENT);
    { const unsigned long long TE = TM + 10000; for (int i = 0; i < 20000; i++) { if (__builtin_amdgcn_s_memrealtime() >= TE) break; __builtin_amdgcn_s_sleep(1); } }
    if (TT == 12345) ws[0] = 0;
}
extern "C" void kernel_launch(void* const* d_in, const int* in_sizes, int n_in,
                              void* d_out, int out_size, void* d_ws, size_t ws_size,
                              hipStream_t stream) {
    fused_kernel<<<256, 512, 0, stream>>>((const char*)d_in[0], (const char*)d_in[1], (const char*)d_in[2], (const char*)d_in[3],
                                          (const char*)d_in[4], (const char*)d_in[5], (const char*)d_in[6], (const char*)d_in[7],
                                          (const char*)d_in[8], (const char*)d_in[9], (char*)d_out, (char*)d_ws);
    tiny2<<<1, 64, 0, stream>>>((char*)d_ws);
}
```

```cpp
#include <hip/hip_runtime.h>
#include <stdint.h>
#include <type_traits>
#pragma clang diagnostic ignored "-Wint-to-pointer-cast"
#if defined(__HIP_DEVICE_COMPILE__) && defined(NOPK)
#pragma clang attribute push(__attribute__((target("no-packed-fp32-ops"))), apply_to = function)
#endif

typedef unsigned v4u __attribute__((ext_vector_type(4)));
typedef unsigned v2u __attribute__((ext_vector_type(2)));
typedef float v16f __attribute__((ext_vector_type(16)));
typedef float v4f __attribute__((ext_vector_type(4)));
typedef float v2f __attribute__((ext_vector_type(2)));
typedef __bf16 v8b __attribute__((ext_vector_type(8)));
typedef __bf16 v2b __attribute__((ext_vector_type(2)));
typedef short v4s __attribute__((ext_vector_type(4)));
#define LAS __attribute__((address_space(3)))
#define DI __device__ __forceinline__
typedef __amdgpu_buffer_rsrc_t rsrc_t;

constexpr unsigned OFF_CF = 8192;
constexpr unsigned OFF_PC = 12288;
constexpr unsigned OFF_H = 16384;
constexpr unsigned OFF_KH = 32768;
constexpr unsigned OFF_YF = 49152;
constexpr unsigned OFF_PART = 1u << 20;
constexpr unsigned OFF_KV = 2u << 20;
constexpr unsigned OFF_YT = (2u << 20) + 262144;
constexpr unsigned OFF_PQ = 4u << 20;
constexpr unsigned OFF_UT = 6u << 20;
constexpr unsigned OFF_UF = 57344;
constexpr unsigned WS_USED = 8u << 20;
constexpr unsigned S_L = 0, S_F = 16, S_E = 24, S_O = 36, S_TAG = 48, S_C = 56, S_A = 64, S_G = 64, S_T = S_G + 16;
constexpr unsigned L_END = 163840;
constexpr unsigned L_X = 256, L_T = L_X + 65536, L_Q = L_END - 57344, OSTR = 912;
constexpr unsigned L_K = 256, KSTR = 4512, L_U = L_K + 16 * KSTR, USTR = 4224, L_TB = L_U + 4 * USTR, TSTR = 2816, L_P = L_TB + 2 * TSTR, L_Y = L_P + 8192;
constexpr unsigned ALL4 = 0x01010101u;
constexpr unsigned SENT = 0xFFFFFFFFu, TAGC = 0x9E3779B1u, FIN = 0xBFFFBFFFu;
#define UA ((l & 1) | ((l >> 1) & 6))
#define UC (((l >> 1) & 1) | ((l >> 3) & 6))
#define GATE 2
#define W1 0
#define W2_ 1
#define W3 4
#define W4 12
#define W5 1
#define PRP 0
#define TU 40
#define TY 60
#define XX 0
#define LD5 8
#define FN 31
#define HS 0
#define UD 2
#define GAL 1
#define CSL -1
#define XD 0
#define PRW 2
#define DK 12
#define XS 1
#define YB 4
#define PRH 3
#define PRK 2
#define PRS 1
#define PRC0 1
#define PRC1 2
#define NB1 2
#define NB2 3
#define WQ 1
#define D1 174
#define D1B 198
#define RP 1
#define KE 1
#define D3 4
#define PRG 1
#define PRL 2
#define GD 3
#define DHH 12
#define PS 1
#define PRB 2
#define GE 4
#define XEND 0
#define CS0 0
#define CS 0
#define CS1 0
#define PC 0
#define YC 0
#define HL 0
#define FQ 4
#define PSL 4
#define PSH 2
#define DKV 277
#define DQ0 296
#define DQ1 318
#define NPRE 16
#define WW 6
#define XW 0
#define LDEL 0
#define LDB 1
#define NW0 0
#define LI 24
#define RB 0
#define TSL 1
#define FP 0
#define CB 0
#define ND 5
#define PP 0
#define OV 0
#define RA 1
#define PGW 0
#define PG0 2
#define DOWN 20
#define DL 0
#define PRU 3
#define KVF 1
#define CL 242
#define ROT xcd
constexpr int OUTAUX = 16;
constexpr int SPIN_L = 1 << 20, SPIN_G = 1 << 14;

#define KST(i)
#define STAMPK(i)
#define STAMP(i)
#define ITC(i, v)

DI float u2f(unsigned v) { return __builtin_bit_cast(float, v); }
DI unsigned f2u(float v) { return __builtin_bit_cast(unsigned, v); }
DI bool allw(bool p) { return __builtin_amdgcn_ballot_w64(!p) == 0; }
DI float bflo(unsigned v) { return u2f(v << 16); }
DI float bfhi(unsigned v) { return u2f(v & 0xffff0000u); }
DI unsigned pk2(float a, float b) { v2f f = {a, b}; return __builtin_bit_cast(unsigned, __builtin_convertvector(f, v2b)); }
DI float rbf(float a) { return bflo(pk2(a, a)); }
DI v16f mfma32(v4u a, v4u b, v16f c) { return __builtin_amdgcn_mfma_f32_32x32x16_bf16(__builtin_bit_cast(v8b, a), __builtin_bit_cast(v8b, b), c, 0, 0, 0); }
DI v4f mfma16(v4u a, v4u b, v4f c) { return __builtin_amdgcn_mfma_f32_16x16x32_bf16(__builtin_bit_cast(v8b, a), __builtin_bit_cast(v8b, b), c, 0, 0, 0); }
DI v4u lr16(unsigned a) { return *(const LAS v4u*)a; }
DI v2u lr8(unsigned a) { return *(const LAS v2u*)a; }
DI unsigned lr4(unsigned a) { return *(const LAS unsigned*)a; }
DI void lw16(unsigned a, v4u v) { *(LAS v4u*)a = v; }
DI void lw8(unsigned a, v2u v) { *(LAS v2u*)a = v; }
DI void lw2(unsigned a, unsigned v) { *(LAS unsigned short*)a = (unsigned short)v; }
DI v2u ltr(unsigned a) { return __builtin_bit_cast(v2u, __builtin_amdgcn_ds_read_tr16_b64_v4i16((LAS v4s*)a)); }
DI v4u ltr2(unsigned a, unsigned b) { v2u x = ltr(a), y = ltr(b); return v4u{x.x, x.y, y.x, y.y}; }
template <int OFS> DI void glds(unsigned long long g, unsigned voff, unsigned lds) {
    asm volatile("s_add_u32 m0, %2, %3\n\ts_nop 0\n\tglobal_load_lds_dwordx4 %0, %1" :: "v"(voff), "s"(g), "s"(lds), "i"(OFS) : "memory", "scc");
}
template <int OFS, int ST, unsigned EH> DI void glds4(unsigned long long g, const unsigned* v, unsigned lds) {
    asm volatile("s_add_u32 m0, %5, %6\n\ts_mov_b32 exec_hi, %8\n\tglobal_load_lds_dwordx4 %0, %4\n\t"
                 "s_add_u32 m0, %5, %6+%7\n\ts_nop 0\n\tglobal_load_lds_dwordx4 %1, %4\n\t"
                 "s_add_u32 m0, %5, %6+2*%7\n\ts_nop 0\n\tglobal_load_lds_dwordx4 %2, %4\n\t"
                 "s_add_u32 m0, %5, %6+3*%7\n\ts_nop 0\n\tglobal_load_lds_dwordx4 %3, %4\n\ts_mov_b32 exec_hi, -1"
                 :: "v"(v[0]), "v"(v[1]), "v"(v[2]), "v"(v[3]), "s"(g), "s"(lds), "i"(OFS), "i"(ST), "i"(EH) : "memory", "scc");
}
template <int OFS, int ST, unsigned EH> DI void glds2(unsigned long long g, const unsigned* v, unsigned lds) {
    asm volatile("s_add_u32 m0, %3, %4\n\ts_mov_b32 exec_hi, %6\n\tglobal_load_lds_dwordx4 %0, %2\n\t"
                 "s_add_u32 m0, %3, %4+%5\n\ts_nop 0\n\tglobal_load_lds_dwordx4 %1, %2\n\ts_mov_b32 exec_hi, -1"
                 :: "v"(v[0]), "v"(v[1]), "s"(g), "s"(lds), "i"(OFS), "i"(ST), "i"(EH) : "memory", "scc");
}
DI void gld(v4u& d, unsigned long long g, unsigned v) { asm volatile("global_load_dwordx4 %0, %1, %2" : "=v"(d) : "v"(v), "s"(g) : "memory"); }
#define VMWAIT(n) asm volatile("s_waitcnt vmcnt(%0)" :: "i"(n) : "memory")
#define CBAR() asm volatile("" ::: "memory")
#define SB() __builtin_amdgcn_sched_barrier(0)
#define SGB() { _Pragma("unroll") for (int q_ = 0; q_ < 4; q_++) { __builtin_amdgcn_sched_group_barrier(8, 1, 0); __builtin_amdgcn_sched_group_barrier(2, 3, 0); } }
#define PIN(x) asm volatile("" : "+v"(x))
#define PINA(x) { PIN(x); __builtin_assume(x < (1u << 24)); }
#define NOUNR _Pragma("clang loop unroll(disable)")
#define WGBAR() asm volatile("s_waitcnt lgkmcnt(0)\n\ts_barrier" ::: "memory")
DI v4u wld16(rsrc_t r, unsigned v, unsigned s) { return __builtin_amdgcn_raw_buffer_load_b128(r, v, s, 16); }
DI v2u wld8(rsrc_t r, unsigned v, unsigned s) { return __builtin_amdgcn_raw_buffer_load_b64(r, v, s, 16); }
DI unsigned wld4(rsrc_t r, unsigned v, unsigned s) { return __builtin_amdgcn_raw_buffer_load_b32(r, v, s, 16); }
template <int AUX> DI void st16(rsrc_t r, unsigned v, unsigned s, v4u d) { __builtin_amdgcn_raw_buffer_store_b128(d, r, v, s, AUX); asm volatile("s_nop 1" :: "v"(d) : "memory"); }
DI void wst16(rsrc_t r, unsigned v, unsigned s, v4u d) { st16<16>(r, v, s, d); }
DI void wst8(rsrc_t r, unsigned v, unsigned s, v2u d) { __builtin_amdgcn_raw_buffer_store_b64(d, r, v, s, 16); }
DI void wst4(rsrc_t r, unsigned v, unsigned s, unsigned d) { __builtin_amdgcn_raw_buffer_store_b32(d, r, v, s, 16); }
template <int OFS> DI void lset8(unsigned a, unsigned v) { asm volatile("s_mov_b64 exec, 1\n\tds_write_b8 %0, %1 offset:%2\n\ts_mov_b64 exec, -1" :: "v"(a), "v"(v), "i"(OFS) : "memory"); }
template <int OFS> DI void lset16(unsigned a, unsigned v) { asm volatile("s_mov_b64 exec, 1\n\tds_write_b16 %0, %1 offset:%2\n\ts_mov_b64 exec, -1" :: "v"(a), "v"(v), "i"(OFS) : "memory"); }
template <int OFS, int SLP = -1, bool W2 = false> DI void lwait(unsigned mask, unsigned base = 0) {
    unsigned x, n;
    v2u t;
#define LW_BODY(RD, MRG) \
    if constexpr (SLP < 0) \
        asm volatile("s_mov_b32 %2, %6\n1:\n\t" RD " offset:%4\n\ts_sub_u32 %2, %2, 1\n\ts_cbranch_scc1 2f\n\ts_waitcnt lgkmcnt(0)\n\t" MRG \
                     "s_andn2_b32 %1, %5, %1\n\ts_cbranch_scc1 1b\n2:\n\ts_waitcnt lgkmcnt(0)" \
                     : "=&v"(T), "=&s"(x), "=&s"(n) : "v"(base), "i"(OFS), "s"(mask), "i"(SPIN_L) : "memory", "scc", "vcc"); \
    else \
        asm volatile("s_mov_b32 %2, %6\n\ts_branch 3f\n1:\n\ts_sleep %7\n3:\n\t" RD " offset:%4\n\ts_sub_u32 %2, %2, 1\n\ts_cbranch_scc1 2f\n\ts_waitcnt lgkmcnt(0)\n\t" MRG \
                     "s_andn2_b32 %1, %5, %1\n\ts_cbranch_scc1 1b\n2:\n\ts_waitcnt lgkmcnt(0)" \
                     : "=&v"(T), "=&s"(x), "=&s"(n) : "v"(base), "i"(OFS), "s"(mask), "i"(SPIN_L), "i"(SLP) : "memory", "scc", "vcc");
    if constexpr (W2) {
        unsigned T;
        LW_BODY("ds_read_b32 %0, %3", "v_readlane_b32 %1, %0, 0\n\tv_readlane_b32 vcc_lo, %0, 1\n\ts_and_b32 %1, %1, vcc_lo\n\t")
    } else {
        unsigned T;
        LW_BODY("ds_read_b32 %0, %3", "v_readfirstlane_b32 %1, %0\n\t")
    }
}
template <int OFS> DI unsigned lpeek(unsigned base = 0) { return *(volatile LAS unsigned*)(base + OFS); }
template <int OFS> DI void lwaitp(unsigned f, unsigned mask, unsigned base = 0) {
    unsigned x, n;
    asm volatile("v_readfirstlane_b32 %1, %0\n\ts_andn2_b32 %1, %5, %1\n\ts_cbranch_scc0 2f\n\ts_mov_b32 %2, %6\n1:\n\tds_read_b32 %0, %3 offset:%4\n\ts_sub_u32 %2, %2, 1\n\ts_waitcnt lgkmcnt(0)\n\t"
                 "s_cbranch_scc1 2f\n\tv_readfirstlane_b32 %1, %0\n\ts_andn2_b32 %1, %5, %1\n\ts_cbranch_scc1 1b\n2:"
                 : "+v"(f), "=&s"(x), "=&s"(n) : "v"(base), "i"(OFS), "s"(mask), "i"(SPIN_L) : "memory", "scc");
}
DI void twait(unsigned t) {
    unsigned n;
    asm volatile("s_movk_i32 %0, 256\n\ts_branch 3f\n1:\n\ts_sleep %2\n3:\n\ts_memrealtime vcc\n\ts_sub_u32 %0, %0, 1\n\ts_waitcnt lgkmcnt(0)\n\ts_cbranch_scc1 2f\n\ts_sub_u32 vcc_lo, vcc_lo, %1\n\ts_cmp_lt_i32 vcc_lo, 0\n\ts_cbranch_scc1 1b\n2:"
                 : "=&s"(n) : "s"(t), "i"(TSL) : "memory", "scc", "vcc");
}
DI void wmax2(int& a, int& b) {
#define WM(C) "v_max_i32_dpp %0, %0, %0 " C "\n\tv_max_i32_dpp %1, %1, %1 " C "\n\ts_nop 0\n\t"
    asm volatile("s_nop 1\n\t" WM("row_shr:1 row_mask:0xf bank_mask:0xf") WM("row_shr:2 row_mask:0xf bank_mask:0xf") WM("row_shr:4 row_mask:0xf bank_mask:0xf") WM("row_shr:8 row_mask:0xf bank_mask:0xf")
                 WM("row_bcast:15 row_mask:0xa bank_mask:0xf") WM("row_bcast:31 row_mask:0xc bank_mask:0xf") : "+v"(a), "+v"(b));
    a = __builtin_amdgcn_readlane(a, 63); b = __builtin_amdgcn_readlane(b, 63);
}
DI unsigned get_tag() {
    lwait<S_TAG, 1>(1);
    return __builtin_amdgcn_readfirstlane(*(volatile LAS unsigned*)(S_TAG + 4));
}
DI void poll(rsrc_t rw, unsigned vo, unsigned so, unsigned tag) {
    NOUNR for (int it = 0; it < SPIN_G; it++) { CBAR(); unsigned f = wld4(rw, vo, so); if (allw(f == tag)) break; }
    CBAR();
}
DI unsigned mx4(v4u a) { return max(max(a.x, a.y), max(a.z, a.w)); }
DI void mxin(unsigned& m, v4u a) { asm volatile("v_max3_u32 %0, %0, %1, %2\n\tv_max3_u32 %0, %0, %3, %4" : "+v"(m) : "v"(a.x), "v"(a.y), "v"(a.z), "v"(a.w)); }
DI v4u nosent(v4u a) { return v4u{min(a.x, SENT - 1), min(a.y, SENT - 1), min(a.z, SENT - 1), min(a.w, SENT - 1)}; }
constexpr float TINIT = 1.000001f, LOG2E = 1.44269504088896f;
DI v4u unit_sw(unsigned a0, unsigned a1, unsigned b0, unsigned b1) {
    auto r0 = __builtin_amdgcn_permlane32_swap(a0, b0, false, false);
    auto r1 = __builtin_amdgcn_permlane32_swap(a1, b1, false, false);
    return v4u{r0[0], r1[0], r0[1], r1[1]};
}
DI v2f cmul(v2f a, v2f b) { v2f t = v2f{a.x, a.x} * b; v2f bs = {-b.y, b.x}; return __builtin_elementwise_fma(v2f{a.y, a.y}, bs, t); }
DI float sc(float x) { asm("" : "+v"(x)); return x; }
DI unsigned pks(float a, float b) { return pk2(sc(a), sc(b)); }
template <bool S> DI v2f cmulx(v2f a, v2f b) {
    if constexpr (!S) return cmul(a, b);
    else { const float x = a.x * b.x, y = a.x * b.y; return v2f{sc(__builtin_fmaf(-a.y, b.y, x)), sc(__builtin_fmaf(a.y, b.x, y))}; }
}
DI v4f add4(v4f a, v4f b) { return v4f{sc(a[0] + b[0]), sc(a[1] + b[1]), sc(a[2] + b[2]), sc(a[3] + b[3])}; }
DI v2f zpow(float a2, float th, float p) {
    float m = __builtin_amdgcn_exp2f(a2 * p), r = __builtin_amdgcn_fractf(th * p);
    return v2f{m * __builtin_amdgcn_cosf(r), m * __builtin_amdgcn_sinf(r)};
}
DI v16f splat16(float v) { return v16f{v, v, v, v, v, v, v, v, v, v, v, v, v, v, v, v}; }
template <int R0> DI void wrows4(unsigned base, const unsigned* a, const unsigned* b) {
    asm volatile("s_mov_b32 m0, %8\n\ts_nop 0\n\t"
                 "ds_write_addtid_b32 %0 offset:%9\n\tds_write_addtid_b32 %4 offset:%9+256\n\t"
                 "ds_write_addtid_b32 %1 offset:%9+528\n\tds_write_addtid_b32 %5 offset:%9+784\n\t"
                 "ds_write_addtid_b32 %2 offset:%9+1056\n\tds_write_addtid_b32 %6 offset:%9+1312\n\t"
                 "ds_write_addtid_b32 %3 offset:%9+1584\n\tds_write_addtid_b32 %7 offset:%9+1840"
                 :: "v"(a[0]), "v"(a[1]), "v"(a[2]), "v"(a[3]), "v"(b[0]), "v"(b[1]), "v"(b[2]), "v"(b[3]), "s"(base), "i"(R0 * 528) : "memory");
}

constexpr int CODE_LINES = CL;
enum { X = 0, K = 8, V = 16, Q = 24, H = 48, W = 56, B = 64 };
constexpr int SCH[] = {X + 0, K + 0, X + 1, K + 1, X + 2, B + K + 0, K + 2, V + 0, B + K + 1, X + 3, K + 3, V + 1, B + V + 0, X + 4, K + 4, V + 2, B + V + 1,
                       X + 5, K + 5, V + 3, B + V + 2, X + 6, K + 6, V + 4, B + V + 3, V + 5, V + 6, B + V + 4, Q + 0, Q + 1, Q + 2, B + V + 6, Q + 3, Q + 4,
H, W, B + Q + FQ};

constexpr int NT = sizeof(SCH) / sizeof(int);
constexpr int tcnt(int c) { return c >= B ? 0 : (c < K ? 4 : (c < H ? 2 : (c < W ? 1 : ND))); }
constexpr int after(int i) { int s = 0; for (int j = i - 1; SCH[j] != SCH[i] - B; j--) s += tcnt(SCH[j]); return s; }
constexpr int nbar(int i) { int s = 0; for (int j = 0; j < i; j++) s += SCH[j] >= B; return s; }
constexpr unsigned KA[7] = {L_Q + 7 * 7168, L_Q + 6 * 7168, L_Q, L_Q + 2 * 7168, L_Q + 4 * 7168, L_T, L_T + 2 * 7168};
constexpr unsigned VA[7] = {L_Q + 7168, L_Q + 3 * 7168, L_Q + 5 * 7168, L_T + 7168, L_T + 3 * 7168, L_Q + 7 * 7168, L_Q + 6 * 7168};
constexpr unsigned L_PK = L_X, L_VV = L_X + 49152, L_ONE = L_T + 4 * 7168;
#define BAR() asm volatile("s_barrier" ::: "memory")
constexpr int opT(int t) { return t < 8 ? K + (t >> 2) : (t < 40 ? (t & 1 ? V + (t >> 3) - 1 : K + (t >> 3) + 1) : (t < 44 ? K + 6 : V + (t >> 2) - 7)); }
constexpr int opM(int t) { return t >= 8 && t < 40 ? (t >> 1) & 3 : t & 3; }
template <int I> using IC = std::integral_constant<int, I>;
template <int A, int E, class F> DI void sfor(F&& f) { if constexpr (A < E) { f(IC<A>{}); sfor<A + 1, E>(f); } }

DI void phik(v16f& a) {
    float ex[16];
#pragma unroll
    for (int j = 0; j < 5; j++) {
        float* e = ex + 4 * j - 4;
#pragma unroll
        for (int i = 0; i < 4; i++) if (j < 4) e[4 + i] = __builtin_fmaf(a[(4 * j + i) & 15], LOG2E, -TINIT * LOG2E);
        SB();
        if (j > 0) asm("v_add_f32 %0, %4, %0\n\tv_add_f32 %1, %4, %1\n\tv_add_f32 %2, %4, %2\n\tv_add_f32 %3, %4, %3" : "+v"(e[0]), "+v"(e[1]), "+v"(e[2]), "+v"(e[3]) : "s"(1e-6f));
        SB();
#pragma unroll
        for (int i = 0; i < 4; i++) {
            if (j < 4) e[4 + i] = __builtin_amdgcn_fmed3f(__builtin_amdgcn_exp2f(e[4 + i]), 0.f, 1.f);
            if (j > 0) e[i] = u2f(max((int)f2u(a[(4 * j - 4 + i) & 15]), (int)f2u(e[i])));
            SB();
        }
        if (j > 0) { a[2 * j - 2] = u2f(pk2(e[0], e[1])); a[2 * j - 1] = u2f(pk2(e[2], e[3])); }
        SB();
    }
    PIN(a);
}

DI void ldq(v4u* pq, rsrc_t rw, unsigned pa, unsigned o) {
#pragma unroll
    for (int i = 0; i < 4; i++) pq[i] = wld16(rw, pa + (3 * (i >> 1) + 2 * (i & 1)) * 512, o);
    CBAR();
}
DI bool mulq(v16f* n, const v4u* kv, const v4u* pq, unsigned m) {
#pragma unroll
    for (int i = 0; i < 4; i++) {
        n[i >> 1] = mfma32(kv[i], pq[i], i & 1 ? n[i >> 1] : splat16(0.f));
        SB();
        mxin(m, pq[i]);
        SB();
    }
    return allw(m != SENT);
}
DI void nrm(const v16f* n, unsigned oa, unsigned o8, bool half) {
    const v16f a = n[0], o = n[1];
    const unsigned rc = f2u(__builtin_amdgcn_rcpf((half ? a[12] : o[0]) + 1e-6f));
    auto r = __builtin_amdgcn_permlane32_swap(rc, rc, false, false);
    const float ro_ = u2f(r[0]), ra = u2f(r[1]), rm = half ? ro_ : ra;
    lw16(oa, v4u{pks(a[0] * ra, a[1] * ra), pks(a[2] * ra, a[3] * ra), pks(a[4] * ra, a[5] * ra), pks(a[6] * ra, a[7] * ra)});
    lw16(oa + 32, v4u{pks(a[8] * ra, a[9] * ra), pks(a[10] * ra, a[11] * ra),
                      pks((half ? o[0] : a[12]) * rm, (half ? o[1] : a[13]) * rm), pks((half ? o[2] : a[14]) * rm, (half ? o[3] : a[15]) * rm)});
    lw16(oa + 64, v4u{pks(o[4] * ro_, o[5] * ro_), pks(o[6] * ro_, o[7] * ro_), pks(o[8] * ro_, o[9] * ro_), pks(o[10] * ro_, o[11] * ro_)});
    lw8(o8 + 192, v2u{pks(o[12] * ro_, o[13] * ro_), pks(o[14] * ro_, o[15] * ro_)});
    SB();
}
constexpr unsigned LO0 = L_X + 28 * 4096;

typedef unsigned long long v8p __attribute__((ext_vector_type(8)));
typedef unsigned long long v4p __attribute__((ext_vector_type(4)));
DI unsigned long long kget(unsigned long long v) { unsigned long long r; asm volatile("s_mov_b64 %0, %1" : "=s"(r) : "s"(v)); return r; }
#define KARR() asm volatile("s_waitcnt lgkmcnt(0)" :: "s"(ka), "s"(kb) : "memory")
#define MKRW() const unsigned long long wsp = kget(kb[3]); const rsrc_t rw = __builtin_amdgcn_make_buffer_rsrc((void*)wsp, 0, WS_USED, 0x00020000)
#define MKRO() const rsrc_t ro = __builtin_amdgcn_make_buffer_rsrc((void*)kget(kb[2]), 0, 2048 * 1792, 0x00020000)
#define EPOCH() { unsigned E; asm volatile("s_load_dword %0, %1, 0\n\ts_waitcnt lgkmcnt(0)" : "=s"(E) : "s"(wsp) : "memory"); tag = E + TAGC; *(volatile LAS v2u*)S_TAG = v2u{1u, tag}; }
#define NOL 0x80000000u

__global__ void __launch_bounds__(768, 3)
fused_kernel(const char* x_, const char* wqkv_, const char* wo_, const char* u_, const char* lar_, const char* aim_, const char* cre_, const char* cim_,
             const char* ldt_, const char* dsk_, char* out_, char* ws_) {
    __shared__ __attribute__((aligned(16))) char smem[L_END];
    const int b = blockIdx.x, tid = threadIdx.x, l = tid & 63;
    asm volatile("Lkstart:" ::: "memory");
    unsigned long long st[16] = {0, 0, 0, 0, 0, 0, 0, 0, 0, 0, 0, 0, 0, 0, 0, 0};
    if ((unsigned)(tid - 256) < 12u) {
        const unsigned iv = l < 8 ? 0u : 0x3F803F80u;
        lw16(l < 8 ? l * 16 : L_ONE - 8 * 2048 + l * 2048, v4u{iv, iv, iv, iv});
        asm volatile("s_waitcnt lgkmcnt(0)" ::: "memory");
    }
    unsigned cv;
    if ((unsigned)(tid - 320) < 64u && b < 128) {
        unsigned long long pc;
        unsigned back;
        asm volatile("s_getpc_b64 %0\nLkpc:\n\ts_mov_b32 %1, Lkpc-Lkstart" : "=s"(pc), "=s"(back));
        asm volatile("global_load_dword %0, %1, %2" : "=v"(cv) : "v"(min(((b >> 3) & 3) * 64 + l, CODE_LINES - 1) * 128), "s"(pc - back) : "memory");
    }
    v8p ka; v4p kb;
    asm volatile("s_load_dwordx16 %0, %2, 0\n\ts_load_dwordx8 %1, %2, 64\n\ts_barrier" : "=&s"(ka), "=&s"(kb) : "s"(__builtin_amdgcn_kernarg_segment_ptr()) : "memory");
    const int w = __builtin_amdgcn_readfirstlane(tid >> 6), q4 = w & 3, gw = w - 4;
    const int xcd = (b & 7) ^ XX, slot = b >> 3;
    asm volatile("" :: "v"(smem));
    const v4u S4 = {SENT, SENT, SENT, SENT};
    unsigned tag;

    if (__builtin_expect(slot < 16, 1)) {
        const int R = 4 * (xcd >> 1) + (slot >> 2), hp = 4 * (xcd & 1) + (slot & 3), ct = slot & 3, c3 = (4 * xcd + (slot >> 2) + 8) & 31;
        const unsigned vq4 = ((l & 7) ^ (((l >> 4) & 1) << 2)) << 4;
        const int hl = w & 1, rh = w >> 3, own = 4 - 4 * (xcd & 1) + (xcd >> 1);
        if (__builtin_expect(w < 4, 1)) {
            __builtin_amdgcn_s_setprio(PRL);
            const unsigned s7 = (l * 37) >> 8;
            unsigned vx[4], vw[2], vqq[2], vq = q4, b2 = (l & 1) * 4;
            PIN(b2);
#pragma unroll
            for (int i = 0; i < 4; i++) { vx[i] = (128 * R + 32 * i + 8 * q4 + (l >> 3)) * 896 + (((l & 7) ^ (4 * (q4 & 1) + (l >> 4))) << 4); PIN(vx[i]); }
#pragma unroll
            for (int i = 0; i < 2; i++) {
                vw[i] = (16 * q4 + 2 * i + 4 * (s7 & 3) + ((s7 >> 2) & 1)) * 2688 + (l - 7 * s7) * 16 + 896 + hp * 112;
                vqq[i] = (16 * q4 + 8 * i + (l >> 3)) * 2688 + vq4 + hp * 128;
                PIN(vw[i]); PIN(vqq[i]);
            }
            unsigned lx = q4 * 1024, lr = q4 * 1792, lq = q4 * 2048, xo[7], ko[7];
            { unsigned r = (ROT); r = r >= 7 ? r - 7 : r;
#pragma unroll
              for (int i = 0; i < 7; i++) { xo[i] = r * 128; ko[i] = r * (64 * 2688); asm volatile("" : "+s"(xo[i]), "+s"(ko[i])); r = r == 6 ? 0 : r + 1; } }
            asm volatile("" : "+v"(vq), "+s"(lx), "+s"(lr), "+s"(lq));
            unsigned vh = q4 == 0 && (l < 4 || (l >> 3) == 1) && l != xcd + 8 ? 0 : NOL;
            vh |= l < 8 ? OFF_H + hp * 512 + l * 128 + R * 8 : OFF_KH + (l - 8) * 1024 + xcd * 128 + slot * 8;
            unsigned vpl = q4 == 1 ? OFF_KH + xcd * 1024 + ((l >> 3) == xcd ? l ^ 8 : l) * 16
                                   : (l & 8 ? OFF_PC + (c3 >> 3) * 512 + (l & 4) * 64 + (c3 & 6) * 32 + (l & 3) * 16 : (l & 4 ? OFF_PC + xcd * 256 + slot * 16 : OFF_CF + (l & 3) * 16));
            unsigned long long tm;
            PIN(vh); PIN(vpl);
            KARR();
            STAMP(1);
            const unsigned long long gx = kget(ka[0]), gk = kget(ka[1]), gv = gk + 896;
            MKRW();
            unsigned long long go;
            unsigned vo2;
            auto iw = [&](auto I) { glds<L_X + I.value * 4096>(go + I.value * 16 * 896, vo2, lx); };
            auto run = [&](auto I, auto& self) {
                constexpr int i = I.value;
                if constexpr (i < NT) {
                    constexpr int c = SCH[i], n = c & 7, ty = c - n;
                    if constexpr (ty == X) glds4<L_X + (n & 3) * 16384, 4096, 0xffffffffu>(gx + xo[n], vx, lx);
                    if constexpr (ty == K) glds2<KA[n], 896, 0xffffffu>(gk + ko[n], vw, lr);
                    if constexpr (ty == V) glds2<VA[n], 896, 0xffffffu>(gv + ko[n], vw, lr);
                    if constexpr (ty == Q) glds2<L_Q + n * 8192, 1024, 0xffffffffu>(gk + ko[n], vqq, lq);
                    if constexpr (ty == H) { asm volatile("s_waitcnt lgkmcnt(0)" : "+s"(tm) :: "memory"); wst8(rw, vh, 0, v2u{tag, (unsigned)tm}); }
                    if constexpr (ty == W) sfor<16, 16 + ND>(iw);
                    if constexpr (i == 1) asm volatile("s_load_dword %0, %1, 0" : "=s"(tag) : "s"(wsp) : "memory");
                    if constexpr (i == 4) { go = kget(ka[2]) + (min(ct * 256 + (q4 & 1) * 128, 768) + (q4 >> 1) * 8 * 896); vo2 = (l >> 3) * 896 + vq4; PIN(vo2); }
                    if constexpr (c >= B) {
                        VMWAIT(after(i));
                        STAMP(2 + nbar(i));
                        BAR();
                        if constexpr (c == (HL ? B + Q + FQ : B + V + 6)) asm volatile("s_memrealtime %0" : "=s"(tm) :: "memory");
                        if constexpr (nbar(i) == 1) { asm volatile("s_waitcnt lgkmcnt(0)" : "+s"(tag) :: "memory"); tag += TAGC; *(volatile LAS v2u*)S_TAG = v2u{1u, tag}; }
                    }
                    self(IC<i + 1>{}, self);
                }
            };
            run(IC<0>{}, run);
            if (XEND == 1) goto done;
            v4u fl;
            glds2<L_Q + 5 * 8192, 1024, 0xffffffffu>(gk + ko[5], vqq, lq);
            glds2<L_Q + 6 * 8192, 1024, 0xffffffffu>(gk + ko[6], vqq, lq);
            sfor<8, 8 + FP>(iw);
            asm volatile("global_load_dwordx4 %0, %1, %2 sc1" : "=v"(fl) : "v"(vpl), "s"(wsp) : "memory");
            sfor<8 + FP, 12>(iw);
            unsigned vg = vq * 4, ym = q4 == 1 ? 0u : ~0u;
            auto good = [&]() { return allw(((fl.x ^ tag) | (fl.z ^ tag) | (((fl.y ^ tag) | (fl.w ^ tag)) & ym)) == 0); };
            auto pub = [&]() {
                const int t0 = (unsigned)tm;
                int tl = min(max(max((int)fl.y - t0, (int)fl.w - t0), -200), 300), tq = (l >> 4) == (c3 >> 3) && (l & 6) == (c3 & 6) ? tl : -200;
                wmax2(tl, tq);
                *(volatile LAS v4u*)(vg * 4 + S_G) = v4u{4u, (unsigned)(max(tl, 0) + t0 + DKV), (unsigned)(tq + t0 + DQ0), 0u};
            };
            sfor<16 + ND, 23>(iw);
            bool ok;
            if (CB) { VMWAIT(11 - ND - FP); PIN(fl); ok = good(); if (ok) pub(); } else VMWAIT(12 - ND);
            STAMP(5);
            BAR();
            __builtin_amdgcn_s_setprio(PRW);
            if (XW == 9) lwait<S_E, 0, true>(ALL4, b2); else if (XW) __builtin_amdgcn_s_sleep(XW);
            sfor<0, 2>(iw);
            if (!CB) { VMWAIT(13 - ND - FP); PIN(fl); ok = good(); if (ok) pub(); }
            sfor<2, 8>(iw); sfor<12, 16>(iw); sfor<23, 28>(iw);
            STAMP(2);
            VMWAIT(5);
            lset8<S_L + 8>(vq, 1);
            VMWAIT(0);
            lset8<S_L + 8>(vq, 3);
            STAMP(3);
            if (!ok && (q4 == 1 || q4 == 2)) {
                NOUNR for (int it = 0; it < SPIN_G; it++) {
                    fl = wld16(rw, vpl, 0);
                    if (good()) break;
                    CBAR();
                    __builtin_amdgcn_s_sleep(PSL);
                }
                pub();
            }
        } else {
            __builtin_amdgcn_s_setprio(PRG);
            const int rg = (gw & 2) + rh;
            unsigned xa[4], vf8 = gw;
#pragma unroll
            for (int m = 0; m < 4; m++) { xa[m] = L_X + (32 * rg + (l & 31)) * 128 + (((2 * m + (l >> 5)) ^ ((l >> 1) & 7)) << 4); PIN(xa[m]); }
            unsigned trw = ((l & 12) + 2 * (l >> 5)) * 112 + hl * 48 + (l & 16) * 2 + (l & 3) * 8 + L_Q;
            const unsigned pso = ((2 * hp + hl) * 8 + 4 * (rg >> 1) + (rg & 1)) * 8192 + R * 512;
            PIN(vf8); PIN(trw);
            KARR();
            STAMP(1);
            MKRW(); MKRO();
            unsigned qs = 2 * c3 * 28672 + 7 * gw * 512, qs1 = qs + 28672, so = gw * 4096, em = ALL4 << rh, dk = gw == own ? DOWN : 0, lo_ = (1 - rh) * (LO0 - L_X), oc = (gw >> 1) * 224 + hl * 8;
            asm volatile("" : "+s"(qs), "+s"(qs1), "+s"(so), "+s"(em), "+s"(dk), "+s"(lo_), "+s"(oc));
            unsigned qso = (4 * R + rg) * 28672 + (8 * hp + 4 * hl) * 512;
            asm volatile("" : "+s"(qso));
            v16f ak = splat16(TINIT), av;
            v4u xf[28], wf[56];
            int l2 = l;
            unsigned ew, pvo, er, er2;
            auto mm_ = [&](auto U) {
                constexpr int u = U.value, c = opT(u), xi = 4 * (c & 7) + opM(u);
                if (c >= V) { av = mfma32(xf[xi], wf[u], u == 9 ? splat16(0.f) : av); PIN(av); }
                else { ak = mfma32(xf[xi], wf[u], ak); PIN(ak); }
                SB();
            };
            auto rd_ = [&](auto T) {
                constexpr int t = T.value, c = opT(t), n = c & 7, m = opM(t);
                if (t == 40) asm volatile("s_waitcnt vmcnt(0)" :: "v"(cv) : "memory");
                if (XEND) { if (t == 8) STAMP(3); if (t == 16) STAMP(11); if (t == 24) STAMP(12); if (t == 32) STAMP(13); if (t == 40) STAMP(14); }
                if (t == 4 || (t <= 40 && !(t & 7))) BAR();
                if (t == 0) STAMP(2);
                if (t == 16 && !XEND) STAMP(3);
                if (t == 40) STAMP(4);
                if (t == 32) { trw -= L_Q - L_T; PIN(trw); }
                if (t == 48) { trw += L_Q - L_T; PIN(trw); }
                if (c < V || n >= 5) xf[4 * n + m] = lr16(xa[m] + (n & 3) * 16384);
                const unsigned ta = c < V ? KA[n] : VA[n], a = trw + (ta - (ta >= L_Q ? L_Q : L_T)) + m * 1792;
                wf[t] = ltr2(a, a + 112);
                SB();
            };
            ew = L_X + (hl * 4 + rg) * 2048 + l * 16; pvo = OFF_PART + (l >> 5) * 16384 + (l & 31) * 16;
            er = L_PK + hl * 8192 + (l >> 4) * 512 + (l & 15) * 16; er2 = er + (rg & 1) * 256 + (L_VV - L_PK);
            er += (rg >> 1) * 256;
            if ((l & 15) + 16 * (rg & 1) == (hl ? 0 : 28)) er2 = L_ONE;
            PIN(ew); PIN(pvo); PIN(er); PIN(er2);
            sfor<0, 44 + GD>([&](auto T) { if constexpr (T.value >= GD) mm_(IC<(T.value >= GD ? T.value - GD : 0)>{}); rd_(T); });
            sfor<44 + GD, 44 + GE>(rd_);
            if (XEND) STAMP(7);
            BAR();
            if (gw < 4) { __builtin_amdgcn_s_setprio(3); phik(ak); __builtin_amdgcn_s_setprio(PRG); }
            else __builtin_amdgcn_s_setprio(PRB);
            if (XEND) STAMP(8);
            sfor<44, 56>([&](auto U) { mm_(U); if constexpr (U.value + GE < 56) rd_(IC<U.value + GE>{}); });
            lw16(ew + (L_VV - L_X), v4u{pk2(av[0], av[1]), pk2(av[2], av[3]), pk2(av[4], av[5]), pk2(av[6], av[7])});
            lw16(ew + (L_VV - L_X) + 1024, v4u{pk2(av[8], av[9]), pk2(av[10], av[11]), pk2(av[12], av[13]), pk2(av[14], av[15])});
            if (XEND) STAMP(9);
            if (gw >= 4) { __builtin_amdgcn_s_setprio(3); phik(ak); __builtin_amdgcn_s_setprio(PRG); }
            lw16(ew + (L_PK - L_X), __builtin_bit_cast(v4u, v4f{ak[0], ak[1], ak[2], ak[3]}));
            lw16(ew + (L_PK - L_X) + 1024, __builtin_bit_cast(v4u, v4f{ak[4], ak[5], ak[6], ak[7]}));
            if (XEND) STAMP(10);
            __builtin_amdgcn_s_setprio(3);
            WGBAR();
            STAMP(5);
            {
                v4u fk[4], fv[4];
#pragma unroll
                for (int i = 0; i < 4; i++) { fk[i] = lr16(er + i * 2048); fv[i] = lr16(er2 + i * 2048); }
                const unsigned vt = lpeek<S_TAG + 4>();
                SB();
                v4f d = {0.f, 0.f, 0.f, 0.f};
#pragma unroll
                for (int i = 0; i < 4; i++) d = mfma16(fk[i], fv[i], d);
                wst16(rw, pvo, pso, v4u{pk2(d[0], d[1]), vt & FIN, pk2(d[2], d[3]), vt & FIN});
                tag = __builtin_amdgcn_readfirstlane(vt);
            }
            __builtin_amdgcn_s_setprio(PRG);
            STAMP(6);
            if (XEND == 1) goto done;
            PIN(l2);
#define l l2
            const int r32 = l & 31, half = l >> 5, c16 = (l >> 4) & 1, cb = (w >> 1) & 1;
            const unsigned trc = ((l >> 2) & 3) * 128 + (((4 * hl + 2 * c16 + ((l >> 1) & 1)) ^ ((l >> 1) & 4)) << 4) + 8 * (l & 1);
            unsigned b2, pa, ka, ob, o8, oa, t4, t5, o80, oa0;
            auto prep = [&]() {
                unsigned l3 = l;
                PIN(l3);
                b2 = (l3 & 1) * 4; pa = OFF_PQ + l3 * 16; ka = OFF_KV + l3 * 16; ob = L_X + (l3 & 31) * OSTR + (l3 >> 5) * 16; o8 = ob + oc; oa = o8 + hl * 88; ob += lo_;
                t4 = trc + (l3 >> 5) * 512 + (L_X + cb * 1024); t5 = t4 + 57344; o80 = o8 + (LO0 - L_X); oa0 = oa + (LO0 - L_X);
                asm volatile("" : "+v"(b2), "+v"(pa), "+v"(ka), "+v"(ob), "+v"(o8), "+v"(oa), "+v"(t4), "+v"(t5), "+v"(o80), "+v"(oa0));
            };
            v4u tt;
            if (__builtin_expect(hp < 7, 1)) {
                const unsigned trb = L_Q + trc + half * 1024;
                float ti = TINIT;
                PIN(ti);
                v16f acc = splat16(ti);
                v4u fa[28];
                unsigned cf;
                constexpr int QP = 4;
#pragma unroll
                for (int i = 0; i < 28 + QP; i++) {
                    if (i >= 4 && i < 8) xf[16 + i] = lr16(xa[i - 4] + 16384);
                    if (i == 20) { if (rh == PGW) __builtin_amdgcn_s_setprio(PG0); BAR(); }
                    if (i < (RA ? 20 : 28)) fa[i] = ltr2(trb + i * 2048, trb + i * 2048 + 512);
                    if (i >= QP) acc = mfma32(fa[i - QP], xf[i - QP], acc);
                    if (i >= 19) PIN(acc);
                    SB();
                    if (i == 20 && RA) {
#pragma unroll
                        for (int j = 20; j < 28; j++) fa[j] = ltr2(trb + j * 2048, trb + j * 2048 + 512);
                    }
                    if (i == (RA ? 20 : 27)) lset8<S_E>(vf8, 1);
                    if (i == 26) prep();
                    if (i == 30) { cf = lpeek<S_G + 32>(); tt = *(volatile LAS v4u*)S_T; }
                    SB();
                }
                STAMP(7);
                __builtin_amdgcn_s_setprio(3);
                phik(acc);
                PIN(tt);
                lwaitp<S_G + 32>(cf, 4);
#pragma unroll
                for (int s = 0; s < 2; s++) {
                    wst16(rw, pa + s * 1024, qso, unit_sw(f2u(acc[4 * s]), f2u(acc[4 * s + 1]), f2u(acc[4 * s + 2]), f2u(acc[4 * s + 3])));
                }
                STAMP(8);
            } else { lset8<S_E>(vf8, 1); prep(); BAR(); __builtin_amdgcn_s_setprio(3); lwait<S_G + 32, 1>(4); tt.x = 0; }
            if (__builtin_expect(__builtin_amdgcn_readfirstlane(tt.x) == 0, 0)) { lwait<S_T, 0>(4); tt = *(volatile LAS v4u*)S_T; }
            v4u of[28];
            auto wof = [&](int ks) {
                const int g = ks / 7, r = ks - 7 * g;
                const unsigned t = (g < 2 ? t4 : t5) + (g & 1) * 112 * 256;
                if (r == 6) return ltr2(t + 48 * 256, t + 104 * 256);
                const int r0 = 56 * (r / 3) + 16 * (r % 3);
                return ltr2(t + r0 * 256, t + (r0 + 8) * 256);
            };
            {
                unsigned tk = __builtin_amdgcn_readfirstlane(tt.y) + dk, tq = __builtin_amdgcn_readfirstlane(tt.z), tq1 = tq + (DQ1 - DQ0);
                asm volatile("" : "+s"(tk), "+s"(tq), "+s"(tq1));
                STAMP(9);
                v16f n[4];
                v4u kv[4], pq[8];
                auto ldk = [&]() {
#pragma unroll
                    for (int i = 0; i < 4; i++) kv[i] = wld16(rw, ka + i * 1024, so);
                    CBAR();
                };
                auto fin = [&]() {
                    unsigned m = 0;
#pragma unroll
                    for (int i = 0; i < 4; i++) mxin(m, kv[i]);
                    SB();
                    return mulq(n, kv, pq, m);
                };
                PINA(pa); PINA(ka);
                twait(tk);
                STAMP(11);
                ldk();
                auto pre = [&]() {
                    lwait<S_L + 8, 1>(ALL4);
#pragma unroll
                    for (int ks = 0; ks < NPRE; ks++) { of[ks] = wof(ks); if (ks == 7) lset8<S_E>(vf8, 3); }
                    SB();
                };
                if (PP == 0) pre();
                twait(tq);
                STAMP(10);
                ldq(pq, rw, pa, qs);
                if (PP == 1) pre();
                if (DQ1 > DQ0) twait(tq1);
                ldq(pq + 4, rw, pa, qs1);
                if (PP == 2) pre();
                if (KE) {
                    unsigned m = 0;
#pragma unroll
                    for (int i = 0; i < 4; i++) mxin(m, kv[i]);
                    if (__builtin_expect(!allw(m != SENT), 0)) { PINA(ka); ldk(); PIN(kv[0]); PIN(kv[1]); PIN(kv[2]); PIN(kv[3]); }
                }
                ITC(3, 0);
                if (__builtin_expect(!fin(), 0))
                    NOUNR for (int it = 0; it < SPIN_G; it++) {
                        PINA(pa); PINA(ka);
                        ldk();
                        ldq(pq, rw, pa, qs);
                        ITC(3, it + 1);
                        if (fin()) break;
                    }
                STAMP(12);
                lwait<S_E, -1, true>(ALL4 * 3, b2);
                bool ok;
                if (OV) ok = mulq(n + 2, kv, pq + 4, 0);
                nrm(n, oa0, o80, half);
                lset8<S_O>(vf8, 1);
                STAMP(1);
                if (!OV) ok = mulq(n + 2, kv, pq + 4, 0);
                ITC(4, 0);
                if (__builtin_expect(!ok, 0))
                    NOUNR for (int it = 0; it < SPIN_G; it++) {
                        PINA(pa);
                        ldq(pq + 4, rw, pa, qs1);
                        ITC(4, it + 1);
                        if (mulq(n + 2, kv, pq + 4, 0)) break;
                    }
                nrm(n + 2, oa, o8, half);
                lset8<S_O>(vf8, 3);
                STAMP(2);
            }
            if (ct < 3 || !cb) {
                lwait<S_L + 8>(ALL4 << 1);
#pragma unroll
                for (int ks = NPRE; ks < 28; ks++) of[ks] = wof(ks);
                lwait<S_O, -1, true>(em, b2);
                STAMP(13);
                if (b == XX && tid == 256) wst4(rw, 0, 0, tag - TAGC + 1);
                v16f acc = splat16(0.f);
                v4u fo[28];
#pragma unroll
                for (int ks = 0; ks < 32; ks++) {
                    if (ks < 28) fo[ks] = lr16(ob + ks * 32);
                    if (ks >= 4) acc = mfma32(of[ks - 4], fo[ks - 4], acc);
                    SB();
                }
                v4u o2[2];
#pragma unroll
                for (int s = 0; s < 2; s++)
                    o2[s] = unit_sw(pk2(acc[8 * s], acc[8 * s + 1]), pk2(acc[8 * s + 2], acc[8 * s + 3]), pk2(acc[8 * s + 4], acc[8 * s + 5]), pk2(acc[8 * s + 6], acc[8 * s + 7]));
#pragma unroll
                for (int i = 0; i < 4; i++) { auto r = __builtin_amdgcn_permlane16_swap(o2[0][i], o2[1][i], false, false); o2[0][i] = r[0]; o2[1][i] = r[1]; }
#pragma unroll
                for (int s = 0; s < 2; s++)
                    st16<OUTAUX>(ro, (l & 15) * 1792 + c16 * 32 + half * 16, (64 * c3 + 32 * rh + 16 * s) * 1792 + ct * 256 + cb * 128 + hl * 64, o2[s]);
                STAMP(14);
            }
#undef l
        }
    } else {
        const int mm = slot - 16, cx = xcd < 7 ? xcd : mm >> 1, cm = xcd < 7 ? mm : 14 + (mm & 1), ch0 = 64 * cx + 4 * cm, nb = 64 - 64 * (xcd & 1) + 16 * (xcd >> 1) + mm, xn = xcd + XS >= 7 ? xcd + XS - 7 : xcd + XS;
        const int half = l >> 5;
        if (w < 4) {
            __builtin_amdgcn_s_setprio(PRH);
            unsigned vq = q4, b2 = (l & 1) * 4;
            v4u s4 = S4, uu[16];
            PIN(vq); PIN(b2); PIN(s4);
            KARR();
            STAMP(1);
            MKRW();
            {
                const rsrc_t rq = __builtin_amdgcn_make_buffer_rsrc((void*)kget(ka[1]), 0, 448 * 2688, 0x00020000);
                const rsrc_t rx = __builtin_amdgcn_make_buffer_rsrc((void*)kget(ka[0]), 0, 2048 * 896, 0x00020000);
                const rsrc_t rwo = __builtin_amdgcn_make_buffer_rsrc((void*)kget(ka[2]), 0, 448 * 896, 0x00020000);
                const rsrc_t ru = __builtin_amdgcn_make_buffer_rsrc((void*)kget(ka[3]), 0, xcd < 7 && q4 < 2 ? 2048 * 896 : 0, 0x00020000);
                const unsigned s7 = l >> 2, wi = mm * 4 + q4;
                const unsigned r0 = (ROT) >= 7 ? (ROT) - 7 : (ROT), c7 = s7 + r0 >= 7 ? s7 + r0 - 7 : s7 + r0, i4 = l >> 3, c4 = i4 + r0 >= 7 ? i4 + r0 - 7 : i4 + r0;
                const unsigned po = l < 28 ? c7 * (64 * 2688) + (l & 3) * 128 + (xcd & 1) * 384 : NOL, px = l < 56 ? (l & 7) * 896 + c4 * 128 : NOL;
                unsigned E;
                if (q4 == 3) asm volatile("s_load_dword %0, %1, 0" : "=s"(E) : "s"(wsp) : "memory");
#define PFK(A, E, O) __builtin_amdgcn_raw_buffer_load_b32(rq, s7 - (A) < (E) - (A) ? po + O : NOL, wi * 2688, 0)
#define PFX(A, E) __builtin_amdgcn_raw_buffer_load_b32(rx, i4 - (A) < (E) - (A) ? px : NOL, (512 * (xcd >> 1) + 8 * wi) * 896, 0)
                const bool up = xcd < 7 && q4 < 2;
                unsigned t[13];
                v4u r[8];
                if (up) {
#pragma unroll
                    for (int i = 0; i < 8; i++) r[i] = __builtin_amdgcn_raw_buffer_load_b128(ru, UA * 7168 + UC * 16 + (i & 3) * 896, (128 * mm + 64 * q4) * 896 + xn * 128 + (i >> 2) * 3584, 0);
                }
                CBAR();
                if (mm < 14) lwait<S_C, -1, true>(ALL4, b2);
                if (xcd == 7 && q4 == 0) {
                    wst16(rw, OFF_KV + (l >> 4) * 4096 + (l & 15) * 16, mm * 256, s4);
                    wst16(rw, OFF_KV + (l >> 4) * 4096 + (l & 15) * 16, mm * 256 + 16384, s4);
                }
                auto batch = [&](int i) {
                    if (XD & 4) { t[3 * i] = t[3 * i + 1] = t[3 * i + 2] = 0; return; }
                    if (i == 0) { t[0] = PFK(PS, 3, 896); t[1] = PFX(PS, 3); t[2] = PFK(0, 1, 1792); }
                    if (i == 1) { t[3] = PFK(3, 5, 896); t[4] = PFX(3, 5); t[5] = PFK(1, 3, 1792); }
                    if (i == 2) { t[6] = PFK(5, 7, 896); t[7] = PFX(5, 7); t[8] = PFK(3, 7, 1792); }
                    CBAR();
                };
#pragma unroll
                for (int i = 0; i < NB1; i++) batch(i);
                if (q4 == 3) {
                    asm volatile("s_waitcnt lgkmcnt(0)" : "+s"(E) :: "memory"); tag = E + TAGC; *(volatile LAS v2u*)S_TAG = v2u{1u, tag};
                }
                if (up) {
#pragma unroll
                    for (int e = 0; e < 8; e++) {
                        const unsigned sel = (e & 1) ? 0x07060302u : 0x05040100u;
                        const int d = e >> 1;
                        wst16(rw, OFF_UT + UC * 32768 + 112 - UA * 16, xn * 262144 + e * 4096 + 3968 - mm * 256 - q4 * 128,
                              v4u{__builtin_amdgcn_perm(r[6][d], r[7][d], sel), __builtin_amdgcn_perm(r[4][d], r[5][d], sel), __builtin_amdgcn_perm(r[2][d], r[3][d], sel), __builtin_amdgcn_perm(r[0][d], r[1][d], sel)});
                    }
                    lset8<S_F>(vq, 1);
                    STAMP(10);
                }
                if (xcd < 7) lwait<S_F, W1>(0x0101);
#pragma unroll
                for (int i = NB1; i < NB2; i++) batch(i);
                if (up) {
                    VMWAIT(XD & 4 ? 0 : 3 * (NB2 - NB1));
                    STAMP(11);
                    if (q4) lset8<S_F>(vq, 3);
                    else {
                        tag = get_tag();
                        lwait<S_F>(0x200);
                        wst4(rw, l == 0 ? OFF_UF : NOL, xn * 128 + mm * 4, tag);
                        lset8<S_F>(vq, 3);
                    }
                }
                if (xcd == 7 && q4 == 0) {
                    tag = get_tag();
                    VMWAIT(3 * NB2);
                    lset8<S_F>(vq, 1);
                    wst4(rw, l == 0 ? OFF_CF : NOL, mm * 4, tag);
                }
                if (xcd < 7) lwait<S_F, W1>(0x02); else __builtin_amdgcn_s_sleep(TU);
#pragma unroll
                for (int i = NB2; i < 3; i++) batch(i);
                CBAR();
                const int xp = (xcd + 7) & 7, hq = 4 * (xp & 1) + (mm & 3);
                auto cq = [&]() {
#pragma unroll
                    for (int j = 0; j < 4; j++) wst16(rw, (hq < 7 ? OFF_PQ + l * 16 : NOL) + j * 1024, (16 * (xp >> 1) + (mm & 12) + q4) * 28672 + hq * 4096, s4);
                };
                if (!(XD & 1)) cq();
                STAMP(9);
                {
                    if (q4 == 2 && mm >= 14) {
                        VMWAIT(0); lset8<S_F>(vq, 1);
                    }
                    else if (q4 == 2) {
                        if (l < 32) lw16(L_U + (l >> 3) * USTR + 4096 + (l & 7) * 16, v4u{0u, 0u, 0u, 0u});
                        tag = get_tag();
                        poll(rw, OFF_UF + (l & 15) * 4, cx * 128, tag);
                        STAMP(12);
#pragma unroll
                        for (int i = 0; i < 16; i++) uu[i] = wld16(rw, OFF_UT + l * 16 + (i & 3) * 1024, (ch0 + (i >> 2)) * 4096);
                        lset8<S_F>(vq, 1);
#pragma unroll
                        for (int i = 0; i < (UD ? 8 : 16); i++) { lw16(L_U + (i >> 2) * USTR + l * 16 + (i & 3) * 1024, uu[i]); if (i == 7) lset8<S_A>(vq, 1); }
                        if (!UD) lset8<S_A>(vq, 3);
                        STAMP(3);
                    } else {
                        if (q4 == 3) {
                            if (xcd < 7) poll(rw, OFF_CF, mm * 4, tag); else lwait<S_F, 1>(1);
                            STAMP(12);
                        }
                        lwait<S_F, W2_>(0x10000);
                    }
                }
                t[9] = __builtin_amdgcn_raw_buffer_load_b32(rq, q4 < 3 ? po : NOL, wi * 2688 + (xcd & 1) * 128, 0);
                t[11] = __builtin_amdgcn_raw_buffer_load_b32(rq, q4 == 0 ? po : NOL, (wi + 3) * 2688 + (xcd & 1) * 128, 0);
                t[10] = __builtin_amdgcn_raw_buffer_load_b32(rwo, l < 49 && q4 < 3 ? l * 128 : NOL, wi * 7 * 896, 0);
                t[12] = __builtin_amdgcn_raw_buffer_load_b32(rwo, l < 49 && q4 == 1 ? l * 128 : NOL, (wi + 2) * 7 * 896, 0);
                CBAR();
                tag = get_tag();
                VMWAIT(4);
                wst4(rw, l < (q4 ? q4 & 1 : 2) ? OFF_PC + l * 8 : NOL, xp * 256 + mm * 16 + q4 * 4, tag);
                STAMP(13);
                if (mm < 14 && q4 < 2) {
                    if (YC) lwait<S_F, 1>(1u << 24);
                    __builtin_amdgcn_s_sleep(CS);
#pragma unroll
                    for (int i = 0; i < 8; i++) if (!(XD & 2)) wst16(rw, OFF_YT + l * 16 + (i & 3) * 1024, (ch0 + 2 * q4 + (i >> 2)) * 4096, s4);
                }
                STAMP(2);
                asm volatile("" :: "v"(t[0]), "v"(t[1]), "v"(t[2]), "v"(t[3]), "v"(t[4]), "v"(t[5]), "v"(t[6]), "v"(t[7]), "v"(t[8]), "v"(t[9]), "v"(t[10]), "v"(t[11]), "v"(t[12]));
            }
            VMWAIT(0);
            if (q4 < 2) lset8<S_A>(vq, 1);
            STAMP(4);
            if (xcd == 7 && q4 < 2) goto done;
            if (XEND == 1) goto done;
            if (q4 == 3) {
                unsigned pa = OFF_PART + l * 16, ko = l < 32 ? OFF_KV + (l & 15) * 16 + (l >> 4) * 8 : NOL, h2 = OFF_H + (l & 15) * 8;
                unsigned keep = (unsigned)(((nb >> 1) & 3) * 8 + ((l >> 4) & 1) * 4 - ((nb >> 3) & 1) * 4) < 28u ? SENT - 1 : 0u;
                const unsigned one = ((l >> 2) & 3) == ((l >> 4) & 1) ? 0x3F80u << (16 * (l & 1)) : 0u;
                v4u sel = {l & 2 ? 0u : one, 0u, l & 2 ? one : 0u, 0u};
                const unsigned tf = tag & FIN;
                PIN(pa); PIN(ko); PIN(h2); PIN(keep); PIN(sel);
                v2u hf;
                NOUNR for (int it = 0; it < SPIN_G; it++) { CBAR(); hf = wld8(rw, h2, (nb >> 4) * 512 + (mm >> 2) * 128); if (allw(hf.x == tag)) break; __builtin_amdgcn_s_sleep(PSH); }
                STAMP(5);
                NOUNR for (int it = 0; it < 64; it++) if (allw((int)((unsigned)__builtin_amdgcn_s_memrealtime() - hf.y) >= D1)) break;
                lset8<S_F>(vq, 1);
                STAMP(10);
                v4f d;
                v4u a[8], a2[8];
                auto ldp = [&](v4u* p) {
                    CBAR(); PIN(pa);
#pragma unroll
                    for (int i = 0; i < 8; i++) p[i] = wld16(rw, pa + (i & 3) * 1024, nb * 8192 + (i >> 2) * 4096);
                    CBAR();
                };
                auto chk = [&](v4u* p) {
                    SB();
                    unsigned m = 0;
                    d = v4f{0.f, 0.f, 0.f, 0.f};
#pragma unroll
                    for (int i = 0; i < 8; i++) { d = mfma16(sel, p[i], d); m |= (p[i].y ^ tf) | (p[i].w ^ tf); PIN(m); SB(); }
                    return allw(m == 0);
                };
                if (RP) {
                ldp(a);
                NOUNR for (int it = 0; it < 64; it++) if (allw((int)((unsigned)__builtin_amdgcn_s_memrealtime() - hf.y) >= D1B)) break;
                NOUNR for (int it = 0; it < SPIN_G; it++) {
                    ldp(a2);
                    ITC(8, 2 * it);
                    if (chk(a)) break;
                    ldp(a);
                    ITC(8, 2 * it + 1);
                    if (chk(a2)) break;
                }
                } else NOUNR for (int it = 0; it < SPIN_G; it++) { ldp(a); ITC(8, it); if (chk(a)) break; }
                STAMP(6);
                wst8(rw, ko, nb * 256, v2u{min(pk2(d[0], d[1]), keep), min(pk2(d[2], d[3]), keep)});
                STAMP(7);
            } else if (q4 == 2) {
                tag = get_tag();
                if (mm < 14) {
                    if (UD) {
                        lwait<S_C + 4, 1>(ALL4 * (UD == 1 ? 64 : 2));
#pragma unroll
                        for (int i = 8; i < 16; i++) lw16(L_U + (i >> 2) * USTR + l * 16 + (i & 3) * 1024, uu[i]);
                        lset8<S_A>(vq, 3);
                    }
                    lwait<S_A, 1>(0x0101);
                    __builtin_amdgcn_s_setprio(PRP);
                    lwait<S_C + 4, W3>(ALL4 * 8);
                    __builtin_amdgcn_s_sleep(W4);
                    lwait<S_C + 4, W5>(ALL4 << YB);
                    lwait<S_C, W5>(ALL4 << YB);
                    __builtin_amdgcn_s_setprio(PRH);
                    wst4(rw, l == 0 ? OFF_YF : NOL, cx * 128 + cm * 4, tag);
                    STAMP(6);
                }
            } else {
                MKRO();
                tag = get_tag();
                unsigned yo = OFF_YT + (l >> 3) * 32768 + (l & 7) * 16, yf = OFF_YF + (l & 15) * 4, oo = 4 * (l & 7) * 1792 + (l >> 3) * 16;
                unsigned yb = xn * 262144 + mm * 256 + q4 * 128, ob = (256 * ((mm & 4) + (mm >> 3)) + 64 * (mm & 3) + 32 * q4) * 1792 + 896 + xn * 128, ob2 = ob + (768 - 512 * (mm >> 3)) * 1792;
                PIN(yo); PIN(yf); PINA(oo);
                asm volatile("" : "+s"(yb), "+s"(ob), "+s"(ob2));
                __builtin_amdgcn_s_setprio(PRP);
                lwait<S_C + 4, W3>(ALL4 << 4);
                lwait<S_C, W3>(ALL4 << 4);
                __builtin_amdgcn_s_setprio(PRH);
                STAMP(5);
                poll(rw, yf, xn * 128, tag);
                __builtin_amdgcn_s_sleep(D3);
                STAMP(6);
                v4u yg[8];
                NOUNR for (int it = 0; it < SPIN_G; it++) { CBAR();
                    unsigned m = 0;
#pragma unroll
                    for (int e = 0; e < 8; e++) yg[e] = wld16(rw, yo, yb + e * 4096);
#pragma unroll
                    for (int e = 0; e < 8; e++) m = max(m, mx4(yg[e]));
                    ITC(9, it);
                    if (allw(m != SENT)) break;
                }
                STAMP(7);
#pragma unroll
                for (int i = 0; i < 8; i++) {
                    const unsigned sel = (i & 1) ? 0x07060302u : 0x05040100u;
                    const int e = i >> 1;
                    st16<OUTAUX>(ro, oo + (i & 1) * 1792, (i >> 2 ? ob2 : ob) + (i & 2) * 1792,
                                 v4u{__builtin_amdgcn_perm(yg[1][e], yg[0][e], sel), __builtin_amdgcn_perm(yg[3][e], yg[2][e], sel),
                                     __builtin_amdgcn_perm(yg[5][e], yg[4][e], sel), __builtin_amdgcn_perm(yg[7][e], yg[6][e], sel)});
                }
                STAMP(8);
            }
        } else if (mm < 14) {
            __builtin_amdgcn_s_setprio(PRK);
            const int g2 = gw >> 2, w4 = q4, chl = q4 & 1, mh = q4 >> 1, ch = ch0 + 2 * g2 + chl;
            const unsigned LK = L_K + g2 * 8 * KSTR, LT = L_TB + 16 + g2 * TSTR;
            unsigned vc = gw, cb4 = 4 * g2;
            unsigned pofs = ch * 512 + mh * 256 + (l & 15) * 16;
            PIN(vc); PIN(cb4); PIN(pofs);
            KARR();
            {
                const unsigned long long a0 = kget(ka[4]), a1 = kget(ka[5]), a2 = kget(ka[6]), a3 = kget(ka[7]);
                const unsigned long long pa = (half ? ((l & 16) ? a3 : a2) : ((l & 16) ? a1 : a0)) + pofs;
                asm volatile("s_mov_b32 m0, %1\n\ts_nop 0\n\tglobal_load_lds_dwordx4 %0, off" :: "v"(pa), "s"(L_P + gw * 1024) : "memory");
                lset8<S_C>(vc, 1);
            }
            STAMP(1);
            MKRW();
            unsigned p_dt, p_d;
            asm volatile("s_load_dword %0, %2, %4\n\ts_load_dword %1, %3, %4" : "=&s"(p_dt), "=&s"(p_d) : "s"(kget(kb[0])), "s"(kget(kb[1])), "s"((ch >> 1) * 4) : "memory");
            unsigned ga[12];
            const int g = l >> 4, lov = (l & 15) + 16 * mh;
            unsigned tw0 = LT + chl * 2 + 4 * (g < 2 ? 4 * g + 8 * lov : (g == 2 ? 256 + 4 * lov : 384 + 2 * lov));
            unsigned tw0b = tw0 + (g == 3 ? 248 : 0);
            unsigned tw1 = LT + chl * 2 + 4 * (g < 2 ? 512 + 128 * g + lov : 673), tw1b = g == 0 ? tw1 : L_Y + gw * 4096 + l * 4;
            unsigned xw = LK + w4 * 8448 + l * 16, xr = LK + (w4 ^ 2) * 8448 + l * 16, ib = LT + 2048 * w4 + 8 * l;
            float fl = (float)(int)(512 * w4 + 2 * l);
            asm volatile("" : "+v"(tw0), "+v"(tw0b), "+v"(tw1), "+v"(tw1b), "+v"(xw), "+v"(xr), "+v"(ib), "+v"(fl));
            *(LAS unsigned*)(LT - 4) = 0u;
            asm volatile("s_waitcnt vmcnt(0) lgkmcnt(0)" : "+s"(p_dt), "+s"(p_d) :: "memory");
            p_dt >>= 16 * chl; p_d >>= 16 * chl;
            const unsigned po = L_P + gw * 1024 + l * 4;
            const unsigned p_ar = lr4(po), p_ai = lr4(po + 256), p_cr = lr4(po + 512), p_ci = lr4(po + 768);
            STAMP(2);
        {
            const float dt = rbf(__expf(bflo(p_dt)));
            v2f z1[2], z8[2], z256[2], cc[2];
#pragma unroll
            for (int m = 0; m < 2; m++) {
                const float ar = -rbf(__expf(m ? bfhi(p_ar) : bflo(p_ar))) * dt, ai = (m ? bfhi(p_ai) : bflo(p_ai)) * dt;
                const float a2 = ar * LOG2E, th = ai * 0.159154943091895f;
                z8[m] = zpow(a2, th, 8.f);
                z1[m] = zpow(a2, -th, 1.f);
                z256[m] = zpow(a2, -th, 256.f);
                cc[m] = v2f{(m ? bfhi(p_cr) : bflo(p_cr)) * dt * 2.f, (m ? bfhi(p_ci) : bflo(p_ci)) * dt * -2.f};
            }
                        const unsigned sb = LK + w4 * 8448, sr = sb + (l & 15) * 528 + (l >> 4) * 16;
            v4u lo[2][8];
            auto rlo = [&](int second) {
#define RL8(B) "ds_read_b128 %" #B "0, %16\n\tds_read_b128 %" #B "1, %16 offset:64\n\tds_read_b128 %" #B "2, %16 offset:128\n\tds_read_b128 %" #B "3, %16 offset:192\n\t" \
               "ds_read_b128 %" #B "4, %16 offset:256\n\tds_read_b128 %" #B "5, %16 offset:320\n\t"
                asm volatile("s_cmp_lg_u32 %17, 0\n\ts_cbranch_scc1 1f\n\t" RL8() "ds_read_b128 %6, %16 offset:384\n\tds_read_b128 %7, %16 offset:448\n\ts_branch 2f\n1:\n\t"
                             RL8(1) "ds_read_b128 %8, %16 offset:384\n\tds_read_b128 %9, %16 offset:448\n2:"
                             : "+v"(lo[0][0]), "+v"(lo[0][1]), "+v"(lo[0][2]), "+v"(lo[0][3]), "+v"(lo[0][4]), "+v"(lo[0][5]), "+v"(lo[0][6]), "+v"(lo[0][7]),
                               "+v"(lo[1][6]), "+v"(lo[1][7]), "+v"(lo[1][0]), "+v"(lo[1][1]), "+v"(lo[1][2]), "+v"(lo[1][3]), "+v"(lo[1][4]), "+v"(lo[1][5])
                             : "v"(sr), "s"(second) : "memory", "scc");
            };
            v2f g0[9], g1[9];
            g0[8] = g1[8] = v2f{1.f, 0.f};
            unsigned pa[8], pb[8];
#define PLO() { SB(); __builtin_amdgcn_s_setprio(PRS); SB(); }
#define PHI() { SB(); __builtin_amdgcn_s_setprio(PRK); SB(); }
#define LOQ(R0, LAST, P) \
            PLO(); g0[0] = g0[8]; g1[0] = g1[8]; \
            _Pragma("unroll") for (int r = 0; r < 8; r++) if (!(LAST && r == 7)) { g0[r + 1] = cmul(g0[r], z8[0]); g1[r + 1] = cmul(g1[r], z8[1]); } \
            PHI(); \
            _Pragma("unroll") for (int r = 0; r < 8; r++) { pa[r] = pk2(g0[r].x, g0[r].y); pb[r] = pk2(g1[r].x, g1[r].y); } \
            wrows4<R0>(sb, pa, pb); wrows4<R0 + 4>(sb, pa + 4, pb + 4);
            KST(9);
            LOQ(0, false, 0)
            KST(10);
            LOQ(8, false, 1)
            rlo(mh ^ 1);
            KST(11);
            LOQ(0, false, 2)
            KST(12);
            LOQ(8, true, 3)
            rlo(mh);
            KST(13);
            v4u fa[8];
            v4f d[2][2];
            d[0][1] = v4f{mh == 0 && l == 0 ? bflo(p_d) : 0.f, 0.f, 0.f, 0.f};
            PIN(d[0][1]);
            auto hst = [&](auto sv) {
            constexpr bool S = decltype(sv)::value;
            v2f hh[2][21];
            unsigned hq[2][24];
            PLO();
            hh[0][0] = cc[0]; hh[1][0] = cc[1];
#pragma unroll
            for (int r = 1; r < 8; r++) { hh[0][r] = cmulx<S>(hh[0][r - 1], z1[0]); hh[1][r] = cmulx<S>(hh[1][r - 1], z1[1]); }
            PHI();
#pragma unroll
            for (int r = 0; r < 16; r++) hq[r >> 3][r & 7] = pk2(hh[r >> 3][r & 7].x, hh[r >> 3][r & 7].y);
            wrows4<0>(sb, hq[0], hq[1]); wrows4<4>(sb, hq[0] + 4, hq[1] + 4);
            PLO();
#pragma unroll
            for (int m = 0; m < 2; m++) {
#pragma unroll
                for (int r = 8; r < 12; r++) hh[m][r] = cmulx<S>(hh[m][2 * (r - 8)], z256[m]);
                hh[m][12] = cmulx<S>(hh[m][8], z256[m]); hh[m][13] = cmulx<S>(hh[m][10], z256[m]);
                hh[m][14] = cmulx<S>(hh[m][12], z256[m]); hh[m][15] = cmulx<S>(hh[m][13], z256[m]);
            }
            PHI();
#pragma unroll
            for (int r = 0; r < 16; r++) hq[r >> 3][8 + (r & 7)] = pk2(hh[r >> 3][8 + (r & 7)].x, hh[r >> 3][8 + (r & 7)].y);
            wrows4<8>(sb, hq[0] + 8, hq[1] + 8); wrows4<12>(sb, hq[0] + 12, hq[1] + 12);
            KST(14);
#pragma unroll
            for (int ks = 0; ks < 8; ks++) fa[ks] = lr16(sr + ks * 64);
            PLO();
#pragma unroll
            for (int r = 16; r < 21; r++) { hh[0][r] = cmulx<S>(hh[0][r == 16 ? 14 : r - 1], z256[0]); hh[1][r] = cmulx<S>(hh[1][r == 16 ? 14 : r - 1], z256[1]); }
            PHI();
#pragma unroll
            for (int m = 0; m < 2; m++)
#pragma unroll
                for (int r = 16; r < 21; r++) hq[m][r] = pk2(hh[m][r].x, hh[m][r].y);
            PIN(hq[1][20]);
            __builtin_amdgcn_s_waitcnt(0xc07f);
            wrows4<0>(sb, hq[0] + 16, hq[1] + 16);
            asm volatile("ds_write_addtid_b32 %0 offset:2112\n\tds_write_addtid_b32 %1 offset:2368" :: "v"(hq[0][20]), "v"(hq[1][20]) : "memory");
            };
            if (HS == 0 || (HS == 1 && !g2)) hst(std::false_type{}); else hst(std::true_type{});
            lset8<S_C>(vc, 65);
            STAMP(3);
            SB();
            v4u fb[8];
            float f[12];
            unsigned cv[6];
            auto gad = [&](int i) {
                if (i < 6) {
#pragma unroll
                    for (int k = 2 * i; k < 2 * i + 2; k++) { f[k] = k == 0 ? fmaxf(fl - 1.f, 0.f) : fl + (float)(128 * (k / 3) - 1 + k % 3); PIN(f[k]); }
                } else if (i < 9) {
#pragma unroll
                    for (int k = 2 * i - 12; k < 2 * i - 10; k++) { cv[k] = pk2(f[2 * k], f[2 * k + 1]); PIN(cv[k]); }
                } else if (i < 15) {
                    asm volatile("v_mad_u32_u16 %0, %2, 4, %3\n\tv_mad_u32_u16 %1, %2, 4, %3 op_sel:[1,0,0,0]" : "=&v"(ga[2 * i - 18]), "=v"(ga[2 * i - 17]) : "v"(cv[i - 9]), "s"(LT - 4 * 0x4280));
                } else if (i < 19) {
                    const int k = i - 15;
                    asm volatile("v_med3_i32 %0, %0, %1, %2" : "+v"(ga[k]) : "v"(ib + 4 * (128 * (k / 3) - 1 + k % 3)), "s"(LT + 512));
                }
            };
#pragma unroll
            for (int i = 0; i < 32; i++) {
                const int ks = i & 7, h = i >> 4, c = (i >> 3) & 1;
                if (i == 16) __builtin_amdgcn_s_waitcnt(0xc07f);
                if (ks || i == 8) asm volatile("v_mfma_f32_16x16x32_bf16 %0, %1, %2, %0" : "+v"(d[h][c]) : "v"(h ? fb[ks] : fa[ks]), "v"(lo[c][ks]));
                else asm volatile("v_mfma_f32_16x16x32_bf16 %0, %1, %2, 0" : "=&v"(d[h][c]) : "v"(h ? fb[ks] : fa[ks]), "v"(lo[c][ks]));
                SB();
                if (i < 8) { fb[i] = lr16(sr + i * 64); CBAR(); }
                if (i == 16) lw16(xw, __builtin_bit_cast(v4u, d[0][0]));
                if (i == 26) { lw16(xw + 1024, __builtin_bit_cast(v4u, d[1][0])); lset8<S_C>(vc, 67); }
                if (i >= 8 && i != 16 && i != 26) gad(i - 8 - (i > 16) - (i > 26));
                SB();
            }
            STAMP(4);
            lwait<S_C, CSL>(2u << (8 * (w4 ^ 2)), cb4);
            STAMPK(9);
            const v4u x0 = lr16(xr), x1 = lr16(xr + 1024);
            PIN(d[1][1]);
            const v4f e0 = add4(__builtin_bit_cast(v4f, x0), d[0][1]), e1 = add4(__builtin_bit_cast(v4f, x1), d[1][1]);
            const unsigned k0 = pk2(e0[0], e0[1]), k1 = pk2(e0[2], e0[3]), k2 = pk2(e1[0], e1[1]), k3 = pk2(e1[2], e1[3]);
            lw2(tw0, k0); lw2(tw0 + 4, k0 >> 16); lw2(tw0b + 8, k1); lw2(tw0b + 12, k1 >> 16);
            lw2(tw1, k2); lw2(tw1b + 128, k2 >> 16); lw2(tw1b + 256, k3); lw2(tw1b + 384, k3 >> 16);
        }
        lset8<S_C>(vc, 71);
        STAMPK(10);
        lwait<S_C, CSL>(ALL4 * 4, cb4);
        STAMPK(11);
        {
            unsigned t[12], zero = 0;
#pragma unroll
            for (int s = 0; s < 12; s++) t[s] = lr4(ga[s]);
            asm volatile("s_mov_b32 m0, %1\n\ts_nop 0\n\tds_write_addtid_b32 %0\n\tds_write_addtid_b32 %0 offset:%3\n\t"
                         "s_mov_b32 m0, %2\n\ts_nop 0\n\tds_write_addtid_b32 %0\n\tds_write_addtid_b32 %0 offset:%3"
                         :: "v"(zero), "s"(LK + 2 * w4 * KSTR), "s"(LK + 2 * w4 * KSTR + 132 - 4 * (w4 & 1)), "i"(KSTR) : "memory");
            unsigned P[16];
            __builtin_amdgcn_s_waitcnt(0xc07f);
#pragma unroll
            for (int i = 0; i < 16; i++) P[i] = __builtin_amdgcn_perm(t[3 * (i >> 2) + (i & 1) + 1], t[3 * (i >> 2) + (i & 1)], i & 2 ? 0x07060302u : 0x05040100u);
#define KW(A, B, C, D, O) "ds_write_addtid_b32 %" #A " offset:" #O "+4\n\tds_write_addtid_b32 %" #B " offset:" #O "+%17+4\n\tds_write_addtid_b32 %" #A " offset:" #O "+2*%17\n\t" \
            "ds_write_addtid_b32 %" #B " offset:" #O "+3*%17\n\tds_write_addtid_b32 %" #C " offset:" #O "+4*%17+4\n\tds_write_addtid_b32 %" #D " offset:" #O "+5*%17+4\n\t" \
            "ds_write_addtid_b32 %" #C " offset:" #O "+6*%17\n\tds_write_addtid_b32 %" #D " offset:" #O "+7*%17\n\t"
            asm volatile("s_mov_b32 m0, %16\n\ts_nop 0\n\t" KW(0, 1, 2, 3, 384) KW(4, 5, 6, 7, 640) KW(8, 9, 10, 11, 896) KW(12, 13, 14, 15, 1152)
                         :: "v"(P[0]), "v"(P[1]), "v"(P[2]), "v"(P[3]), "v"(P[4]), "v"(P[5]), "v"(P[6]), "v"(P[7]), "v"(P[8]), "v"(P[9]), "v"(P[10]), "v"(P[11]),
                            "v"(P[12]), "v"(P[13]), "v"(P[14]), "v"(P[15]), "s"(LK + 1024 * w4), "i"(KSTR) : "memory");
        }
        lset8<S_C>(vc, 79);
        STAMP(5);
        {
            unsigned ab = LK + (chl * 4 + (l & 3)) * KSTR + 128 * ((l >> 2) & 3) + 16 * (l >> 4) + 256 * mh;
            unsigned bb = L_U + (2 * g2 + chl) * USTR + 2 * (2076 - 224) - 3584 - 8 * (l & 15) + 16 * (l >> 4) + 256 * mh, bb8 = bb + 8;
            unsigned yw = L_Y + gw * 4096 + l * 16, yr = L_Y + (gw ^ 2) * 4096 + l * 16, yo = OFF_YT + l * 16;
            unsigned ab3 = ab - 512 * mh, ab4 = ab + 3584 * mh, f95 = 95, fc = 127, vz = 0, fa_, ys = ch * 4096 + mh * 2048;
            PIN(ab); PIN(bb); PIN(bb8); PIN(yw); PIN(yr); PIN(yo); PIN(ab3); PIN(ab4); PIN(f95); PIN(fc); PIN(vz);
            asm volatile("" : "+s"(ys));
            v4f y[8];
            v4u A[4][8];
            v2u B[4][16];
            auto ld = [&](int rho, int j) {
                if (j < 16) B[rho][j] = lr8((j & 1 ? bb8 : bb) + 512 * (7 - (j >> 1)) + 64 * rho); else A[rho][j - 16] = lr16((rho < 3 ? ab : j > 16 ? ab3 : ab4) + 512 * (j - 16) + 64 * rho);
                CBAR();
            };
            auto u4 = [&](int rho, int bq) { return v4u{B[rho][2 * bq].x, B[rho][2 * bq].y, B[rho][2 * bq + 1].x, B[rho][2 * bq + 1].y}; };
            auto mf = [&](int rho, int a, int bq) {
                if (rho || a) asm volatile("v_mfma_f32_16x16x32_bf16 %0, %1, %2, %0" : "+v"(y[a + bq]) : "v"(A[rho][a]), "v"(u4(rho, bq)));
                else asm volatile("v_mfma_f32_16x16x32_bf16 %0, %1, %2, 0" : "=&v"(y[a + bq]) : "v"(A[rho][a]), "v"(u4(rho, bq)));
                SB();
            };
            lwait<S_A, 1>(0x10000u << g2);
#pragma unroll
            for (int j = 0; j < 16; j++) ld(0, j);
            lwait<S_C, CSL>(ALL4 * 8, cb4);
            STAMP(6);
#pragma unroll
            for (int j = 16; j < 24; j++) ld(0, j);
            if (GATE) lwait<S_C + 4, 1>((GATE == 1 ? 64u : GATE) << (8 * w4));
            asm volatile("s_setprio %1\n\ts_cmp_eq_u32 %0, 0\n\ts_cbranch_scc1 1f\n\ts_setprio %2\n1:" :: "s"(g2), "i"(PRC0), "i"(PRC1) : "scc");
            __builtin_amdgcn_s_waitcnt(0xc07f);
            SB();
#pragma unroll
            for (int rho = 0; rho < 3; rho++) {
                int n = 0;
#pragma unroll
                for (int a = 0; a < 8; a++)
#pragma unroll
                    for (int bq = 7 - a; bq >= 0; bq--) {
                        mf(rho, a, bq);
                        const int j = n - n / LD5;
                        if (n % LD5 < LD5 - 1 && j < 24) ld(rho + 1, j);
                        if (rho == 2 && n == FN) lset8<S_C>(vc, f95);
                        if (rho == 2 && n == FN - 1) fa_ = lpeek<S_A>(vz);
                        SB();
                        n++;
                    }
                __builtin_amdgcn_s_waitcnt(0xc07f);
            }
#define MF(D, A_, B_, C) "v_mfma_f32_16x16x32_bf16 %" #D ", %" #A_ ", %" #B_ ", %" #C "\n\t"
#define YW(R, O) "ds_write_b128 %25, %" #R " offset:" #O "\n\t"
#define YF "s_mov_b64 exec, 1\n\tds_write_b8 %27, %8 offset:%29\n\ts_mov_b64 exec, -1\n\t"
#define YP "ds_read_b32 %8, %28 offset:%29\n\t"
            asm volatile("s_cmp_lg_u32 %26, 0\n\ts_cbranch_scc1 1f\n\t"
                         MF(1, 9, 18, 1) MF(1, 10, 17, 1) MF(2, 9, 19, 2) MF(2, 10, 18, 2) MF(2, 11, 17, 2) MF(5, 9, 22, 5) MF(5, 10, 21, 5) MF(5, 11, 20, 5) MF(5, 12, 19, 5)
                         MF(5, 13, 18, 5) MF(5, 14, 17, 5) MF(6, 9, 23, 6) MF(6, 10, 22, 6) YW(1, 0) MF(6, 11, 21, 6) MF(6, 12, 20, 6) YW(2, 1024) MF(6, 13, 19, 6) MF(6, 14, 18, 6)
                         MF(6, 15, 17, 6) YW(5, 2048) MF(0, 9, 17, 0) MF(3, 9, 20, 3) MF(3, 10, 19, 3) MF(3, 11, 18, 3) YW(6, 3072) MF(3, 12, 17, 3) YF MF(4, 9, 21, 4) MF(4, 10, 20, 4)
                         MF(4, 11, 19, 4) MF(4, 12, 18, 4) YP MF(4, 13, 17, 4) MF(7, 9, 24, 7) MF(7, 10, 23, 7)
                         "s_branch 2f\n1:\n\t"
                         MF(0, 10, 17, 0) MF(3, 10, 20, 3) MF(3, 11, 19, 3) MF(3, 12, 18, 3) MF(3, 13, 17, 3) MF(4, 10, 21, 4) MF(4, 11, 20, 4) MF(4, 12, 19, 4) MF(4, 13, 18, 4)
                         MF(4, 14, 17, 4) MF(7, 10, 24, 7) MF(7, 11, 23, 7) MF(7, 12, 22, 7) YW(0, 0) MF(7, 13, 21, 7) MF(7, 14, 20, 7) YW(3, 1024) MF(7, 15, 19, 7) MF(7, 16, 18, 7)
                         MF(7, 9, 17, 7) YW(4, 2048) MF(1, 10, 18, 1) MF(0, 11, 17, 1) MF(2, 10, 19, 2) MF(2, 11, 18, 2) YW(7, 3072) MF(3, 12, 17, 2) YF MF(5, 10, 22, 5) MF(5, 11, 21, 5)
                         MF(5, 12, 20, 5) MF(5, 13, 19, 5) YP MF(5, 14, 18, 5) MF(4, 15, 17, 5) MF(7, 10, 23, 6)
                         "2:\n\ts_waitcnt lgkmcnt(0)"
                         : "+v"(y[0]), "+v"(y[1]), "+v"(y[2]), "+v"(y[3]), "+v"(y[4]), "+v"(y[5]), "+v"(y[6]), "+v"(y[7]), "+v"(fc)
                         : "v"(A[3][0]), "v"(A[3][1]), "v"(A[3][2]), "v"(A[3][3]), "v"(A[3][4]), "v"(A[3][5]), "v"(A[3][6]), "v"(A[3][7]),
                           "v"(u4(3, 0)), "v"(u4(3, 1)), "v"(u4(3, 2)), "v"(u4(3, 3)), "v"(u4(3, 4)), "v"(u4(3, 5)), "v"(u4(3, 6)), "v"(u4(3, 7)),
                           "v"(yw), "s"(mh), "v"(vc), "v"(cb4), "i"(S_C) : "memory", "scc");
            SB();
            lwaitp<S_C>(fc, 32u << (8 * (w4 ^ 2)), cb4);
            v4f k[4];
#pragma unroll
            for (int a = 2; a < 8; a++) {
                if (a < 6) { k[a - 2] = __builtin_bit_cast(v4f, lr16(yr + (a - 2) * 1024)); CBAR(); }
                mf(3, a, 7 - a);
                if (a == 5) lwaitp<S_A>(fa_, 0x0101, vz);
            }
            STAMP(7);
            __builtin_amdgcn_s_setprio(3);
            k[0] = add4(k[0], y[0]); k[1] = add4(k[1], y[3]); k[2] = add4(k[2], y[4]);
            PIN(k[0]); PIN(k[1]); PIN(k[2]); PIN(y[7]);
            k[3] = add4(k[3], y[7]);
#pragma unroll
            for (int i = 0; i < 2; i++)
                wst16(rw, yo + i * 1024, ys,
                      nosent(v4u{pk2(k[2 * i][0], k[2 * i][1]), pk2(k[2 * i][2], k[2 * i][3]), pk2(k[2 * i + 1][0], k[2 * i + 1][1]), pk2(k[2 * i + 1][2], k[2 * i + 1][3])}));
            STAMP(8);
        }
        } else if (gw == 0) {
            lwait<S_F, 8>(1u << 24);
            __builtin_amdgcn_s_sleep(TY);
            *(volatile LAS v2u*)S_C = v2u{~0u, ~0u};
        }
    }
done:;
}

#if defined(__HIP_DEVICE_COMPILE__) && defined(NOPK)
#pragma clang attribute pop
#endif
extern "C" void kernel_launch(void* const* d_in, const int* in_sizes, int n_in,
                              void* d_out, int out_size, void* d_ws, size_t ws_size,
                              hipStream_t stream) {
    fused_kernel<<<256, 768, 0, stream>>>((const char*)d_in[0], (const char*)d_in[1], (const char*)d_in[2], (const char*)d_in[3],
                                          (const char*)d_in[4], (const char*)d_in[5], (const char*)d_in[6], (const char*)d_in[7],
                                          (const char*)d_in[8], (const char*)d_in[9], (char*)d_out, (char*)d_ws);
}
```
